```python
import torch

def _softmax(x, axis=-1):
    return torch.softmax(x, dim=axis)
def _gelu(x):
    return torch.nn.functional.gelu(x, approximate="tanh")
def _silu(x):
    return torch.nn.functional.silu(x)
def _sigmoid(x):
    return torch.sigmoid(x)
def _layernorm(x, axis=-1, eps=1e-5):
    m = x.mean(dim=axis, keepdim=True)
    v = x.var(dim=axis, keepdim=True, unbiased=False)
    return (x - m) / torch.sqrt(v + eps)
def _scatter_add(tgt, idx, src, axis=0):
    return tgt.clone().index_add_(axis, idx, src)
def _segment_sum(src, seg, n):
    out = torch.zeros((n,) + src.shape[1:], dtype=src.dtype)
    return out.index_add_(0, seg, src)
def _pad(x, pw):
    flat = tuple(v for pair in reversed(pw) for v in pair)
    return torch.nn.functional.pad(x, flat)

kernel_name = 'parallel_sum__sliding_local_dilated__glu_gated_swiglu__prompt'

def setup_inputs():

    _w = (lambda a: (lambda t: t.to(torch.bfloat16) if t.is_floating_point() else t)(torch.as_tensor(a)))
    def _A_setup():
        B, S, D = 1, 2048, 640
        return {
            "x": _w((lambda *s: torch.randn(*s, dtype=torch.bfloat16) * 0.05)(B, S, D)),
            "wqkv": _w((lambda *s: torch.randn(*s, dtype=torch.bfloat16) * 0.05)(D, 3 * D)),
            "wo": _w((lambda *s: torch.randn(*s, dtype=torch.bfloat16) * 0.05)(D, D)),
        }
    _A_in = _A_setup()

    def _B_setup():
        B, S, D, F = 1, 2048, 640, 1728
        return {
            "x": _w((lambda *s: torch.randn(*s, dtype=torch.bfloat16) * 0.05)(B, S, D)),
            "w1": _w((lambda *s: torch.randn(*s, dtype=torch.bfloat16) * 0.8)(D, F) * (2.0 / D) ** 0.5),
            "w3": _w((lambda *s: torch.randn(*s, dtype=torch.bfloat16) * 0.8)(D, F) * (2.0 / D) ** 0.5),
            "w2": _w((lambda *s: torch.randn(*s, dtype=torch.bfloat16) * 0.8)(F, D) * (2.0 / F) ** 0.5),
        }
    _B_in = _B_setup()

    return {
        "A_x": _A_in["x"],
        "A_wqkv": _A_in["wqkv"],
        "A_wo": _A_in["wo"],
        "B_x": _B_in["x"],
        "B_w1": _B_in["w1"],
        "B_w3": _B_in["w3"],
        "B_w2": _B_in["w2"],
    }

def reference(A_x, A_wqkv, A_wo, B_x, B_w1, B_w3, B_w2):
    def _A_ref(x, wqkv, wo):
        B, S, D = x.shape
        H = 32; Dh = D // H
        W, dil = 8, 2
        qkv = (lambda x, *d: x.permute(*d))((x @ wqkv).reshape(B, S, 3, H, Dh), 2, 0, 3, 1, 4)
        q, k, v = qkv[0], qkv[1], qkv[2]
        scale = Dh ** -0.5
        pos = torch.arange(S)
        off = torch.arange(W) * dil
        idx = pos[:, None] - off[None, :]
        mask = idx >= 0
        idx_c = torch.where(mask, idx, 0).reshape(-1)
        kg = k[:, :, idx_c, :].reshape(B, H, S, W, Dh)
        vg = v[:, :, idx_c, :].reshape(B, H, S, W, Dh)
        logits = torch.einsum("bhsd,bhswd->bhsw", q, kg) * scale
        logits = torch.where(mask[None, None, :, :], logits, -1e9)
        attn = _softmax(logits, axis=-1)
        o = torch.einsum("bhsw,bhswd->bhsd", attn, vg)
        return (lambda x, *d: x.permute(*d))(o, 0, 2, 1, 3).reshape(B, S, D) @ wo

    def _B_ref(x, w1, w3, w2):
        gate = torch.einsum("bsd,df->bsf", x, w1)
        up = torch.einsum("bsd,df->bsf", x, w3)
        h = _silu(gate) * up
        out = torch.einsum("bsf,fd->bsd", h, w2)
        return out

    x = A_x
    xn = _layernorm(x, axis=-1)
    return x + _A_ref(xn, A_wqkv, A_wo) + _B_ref(xn, B_w1, B_w3, B_w2)
```

```cpp
#include <hip/hip_runtime.h>
#include <hip/hip_bf16.h>
typedef __hip_bfloat16 bf16;
__device__ __forceinline__ float b2f(bf16 v){ return __bfloat162float(v); }

__global__ void ln_k(const bf16* x, float* xn){
  int r = blockIdx.x; int t = threadIdx.x;
  __shared__ float red[256];
  float s=0; for(int i=t;i<640;i+=256) s+=b2f(x[r*640+i]);
  red[t]=s; __syncthreads(); for(int o=128;o>0;o>>=1){ if(t<o) red[t]+=red[t+o]; __syncthreads(); }
  float m=red[0]/640.f; __syncthreads();
  float v=0; for(int i=t;i<640;i+=256){ float d=b2f(x[r*640+i])-m; v+=d*d; }
  red[t]=v; __syncthreads(); for(int o=128;o>0;o>>=1){ if(t<o) red[t]+=red[t+o]; __syncthreads(); }
  float rs=rsqrtf(red[0]/640.f+1e-5f);
  for(int i=t;i<640;i+=256) xn[r*640+i]=(b2f(x[r*640+i])-m)*rs;
}
__global__ void gemm_k(const float* A, const bf16* W, float* C, int K, int N){
  int n = blockIdx.x*256+threadIdx.x; int r = blockIdx.y;
  if(n>=N) return;
  float s=0; for(int k=0;k<K;k++) s+=A[r*K+k]*b2f(W[(size_t)k*N+n]);
  C[(size_t)r*N+n]=s;
}
__device__ __forceinline__ float rb(float v){ return __bfloat162float(__float2bfloat16(v)); }
__global__ void attn_k(const float* qkv, float* o){
  int r = blockIdx.x; int h = threadIdx.x; if(h>=32) return;
  float lg[8]; float mx=-1e30f;
  for(int w=0;w<8;w++){ int j=r-2*w; if(j<0){lg[w]=-1e9f;} else { float s=0; for(int d=0;d<20;d++) s+=rb(qkv[(size_t)r*1920+h*20+d])*rb(qkv[(size_t)j*1920+640+h*20+d]); lg[w]=s*0.22360679775f; } mx=fmaxf(mx,lg[w]); }
  float sum=0; for(int w=0;w<8;w++){ lg[w]=__expf(lg[w]-mx); sum+=lg[w]; }
  for(int d=0;d<20;d++){ float s=0; for(int w=0;w<8;w++){ int j=r-2*w; if(j<0) j=0; s+=rb(lg[w]/sum)*rb(qkv[(size_t)j*1920+1280+h*20+d]); } o[(size_t)r*640+h*20+d]=rb(s); }
}
__global__ void glu_k(const float* g, const float* u, float* h){
  int i = blockIdx.x*256+threadIdx.x; float a=g[i]; h[i]=rb(a/(1.f+__expf(-a))*u[i]);
}
__global__ void fin_k(const bf16* x, const float* a, const float* b, bf16* out){
  int i = blockIdx.x*256+threadIdx.x; out[i]=__float2bfloat16(b2f(x[i])+a[i]+b[i]);
}
extern "C" void kernel_launch(void* const* d_in, const int* in_sizes, int n_in, void* d_out, int out_size, void* d_ws, size_t ws_size, hipStream_t stream){
  const bf16* x=(const bf16*)d_in[0]; const bf16* wqkv=(const bf16*)d_in[1]; const bf16* wo=(const bf16*)d_in[2];
  const bf16* w1=(const bf16*)d_in[4]; const bf16* w3=(const bf16*)d_in[5]; const bf16* w2=(const bf16*)d_in[6];
  float* ws=(float*)d_ws; float* xn=ws; float* qkv=xn+2048*640; float* o=qkv+2048*1920; float* g=o+2048*640; float* u=g+2048*1728; float* h=u+2048*1728; float* a=h+2048*1728; float* b=a+2048*640;
  ln_k<<<2048,256,0,stream>>>(x,xn);
  gemm_k<<<dim3(8,2048),256,0,stream>>>(xn,wqkv,qkv,640,1920);
  attn_k<<<2048,64,0,stream>>>(qkv,o);
  gemm_k<<<dim3(7,2048),256,0,stream>>>(xn,w1,g,640,1728);
  gemm_k<<<dim3(7,2048),256,0,stream>>>(xn,w3,u,640,1728);
  glu_k<<<2048*1728/256,256,0,stream>>>(g,u,h);
  gemm_k<<<dim3(3,2048),256,0,stream>>>(o,wo,a,640,640);
  gemm_k<<<dim3(3,2048),256,0,stream>>>(h,w2,b,1728,640);
  fin_k<<<2048*640/256,256,0,stream>>>(x,a,b,(bf16*)d_out);
}
```

```cpp
#include <hip/hip_runtime.h>
#include <hip/hip_bf16.h>
#include <utility>

typedef short    s16x4 __attribute__((ext_vector_type(4)));
typedef __bf16   bf16x2 __attribute__((ext_vector_type(2)));
typedef __bf16   bf16x8 __attribute__((ext_vector_type(8)));
typedef float    f32x2 __attribute__((ext_vector_type(2)));
typedef float    f32x4 __attribute__((ext_vector_type(4)));
typedef unsigned u32x2 __attribute__((ext_vector_type(2)));
typedef unsigned u32x4 __attribute__((ext_vector_type(4)));
typedef unsigned long long u64;
typedef __amdgpu_buffer_rsrc_t rsrc_t;
#define LDSP __attribute__((address_space(3)))
#define GAS __attribute__((address_space(1)))
#define DI __device__ __forceinline__
#define GAP() __builtin_amdgcn_sched_barrier(0)
template <int V> using IC = std::integral_constant<int, V>;
template <class F, int... I> DI void sfor_(F&& f, std::integer_sequence<int, I...>) { (f(IC<I>{}), ...); }
template <int N, class F> DI void sfor(F&& f) { sfor_(f, std::make_integer_sequence<int, N>{}); }

#define STAMPS 0
#define ABL 0

constexpr unsigned WS_E  = 0;
constexpr unsigned WS_FH = 4096;
constexpr unsigned WS_FW = 20480;
constexpr unsigned WS_GR = 32768;
constexpr unsigned WS_GC = 65536;
constexpr unsigned CTL_BYTES = WS_GC + 3u * 5376u * 8u;
constexpr unsigned WS_H = 1u << 20;
constexpr unsigned H_BYTES = 32u * 74u * 4096u;
constexpr unsigned WS_W = 16u << 20;
constexpr unsigned W_BYTES = 8u * 74u * 5120u;
constexpr unsigned WS_ST = 32u << 20;
constexpr unsigned OOB = 0x7f000000u;
constexpr int LDS_BYTES = 163840;
constexpr int SC1 = 16;
constexpr int SPIN_CAP = 1 << 18;
constexpr int CODE_KB = 46;

struct Params {
  const char* x; const char* wqkv; const char* wo; const char* w1; const char* w3; const char* w2;
  char* out; char* ws;
};

DI const char* sel(bool c, const char* a, const char* b) { return c ? a : b; }
DI unsigned rfl(unsigned v) { return __builtin_amdgcn_readfirstlane(v); }
DI rsrc_t mk(const void* p, unsigned bytes) { return __builtin_amdgcn_make_buffer_rsrc((void*)p, (short)0, (int)bytes, 0x00020000); }
DI void dma(rsrc_t r, unsigned voff, unsigned soff, unsigned lds) {
  asm volatile("s_mov_b32 m0, %0\n\ts_nop 0\n\tbuffer_load_dwordx4 %1, %2, %3 offen lds" :: "s"(lds), "v"(voff), "s"(r), "s"(soff) : "memory");
}
DI void dma_dev(rsrc_t r, unsigned voff, unsigned soff, unsigned lds) {
  asm volatile("s_mov_b32 m0, %0\n\ts_nop 0\n\tbuffer_load_dwordx4 %1, %2, %3 offen sc1 lds" :: "s"(lds), "v"(voff), "s"(r), "s"(soff) : "memory");
}
DI u32x4 hload4(rsrc_t r, unsigned voff, unsigned soff) { u32x4 v; asm volatile("buffer_load_dwordx4 %0, %1, %2, %3 offen" : "=v"(v) : "v"(voff), "s"(r), "s"(soff) : "memory"); return v; }
DI unsigned hload1(rsrc_t r, unsigned voff, unsigned soff) { unsigned v; asm volatile("buffer_load_dword %0, %1, %2, %3 offen" : "=v"(v) : "v"(voff), "s"(r), "s"(soff) : "memory"); return v; }
template <class T> DI void land(T& v) { asm volatile("" : "+v"(v) :: "memory"); }
template <int N> DI void wait_bar() { asm volatile("s_waitcnt vmcnt(%0) lgkmcnt(0)\n\ts_barrier" :: "i"(N) : "memory"); }
template <int N> DI u64 first_bar(const char* e) {
  u64 v; asm volatile("s_load_dwordx2 %0, %1, 0x0\n\ts_waitcnt vmcnt(%2) lgkmcnt(0)\n\ts_barrier" : "=&s"(v) : "s"(e), "i"(N) : "memory"); return v;
}
DI void lds_bar() { asm volatile("s_waitcnt lgkmcnt(0)\n\ts_barrier" ::: "memory"); }
DI unsigned opaque(unsigned v) { asm volatile("" : "+v"(v)); return v; }
DI bf16x8 ld128(unsigned a) { return *(const LDSP bf16x8*)(size_t)a; }
DI u32x4  ld128u(unsigned a) { return *(const LDSP u32x4*)(size_t)a; }
DI f32x4  ld128f(unsigned a) { return *(const LDSP f32x4*)(size_t)a; }
DI u32x2  ld64(unsigned a) { return *(const LDSP u32x2*)(size_t)a; }
DI f32x2  ld64f(unsigned a) { return *(const LDSP f32x2*)(size_t)a; }
DI float  ld32f(unsigned a) { return *(const LDSP float*)(size_t)a; }
DI u32x2  tr64(unsigned a) { s16x4 v = __builtin_amdgcn_ds_read_tr16_b64_v4i16((LDSP s16x4*)(size_t)a); return __builtin_bit_cast(u32x2, v); }
DI bf16x8 cat(u32x2 a, u32x2 b) { u32x4 v = {a.x, a.y, b.x, b.y}; return __builtin_bit_cast(bf16x8, v); }
DI bf16x8 asb(u32x4 v) { return __builtin_bit_cast(bf16x8, v); }
DI f32x4 mfma(bf16x8 a, bf16x8 b, f32x4 c) { return __builtin_amdgcn_mfma_f32_16x16x32_bf16(a, b, c, 0, 0, 0); }
DI unsigned pk(float lo, float hi) { f32x2 v = {lo, hi}; bf16x2 b = __builtin_convertvector(v, bf16x2); return __builtin_bit_cast(unsigned, b); }
DI float asf(unsigned u) { return __builtin_bit_cast(float, u); }
DI unsigned asu(float f) { return __builtin_bit_cast(unsigned, f); }
DI f32x2 fma2(f32x2 a, f32x2 b, f32x2 c) { return __builtin_elementwise_fma(a, b, c); }
DI f32x2 lo2(f32x4 v) { return __builtin_shufflevector(v, v, 0, 1); }
DI f32x2 hi2(f32x4 v) { return __builtin_shufflevector(v, v, 2, 3); }
DI f32x2 bc2(float v) { return (f32x2){v, v}; }
DI unsigned pk2(f32x2 v) { bf16x2 b = __builtin_convertvector(v, bf16x2); return __builtin_bit_cast(unsigned, b); }
DI float max3(float a, float b, float c) { float r; asm("v_max3_f32 %0, %1, %2, %3" : "=v"(r) : "v"(a), "v"(b), "v"(c)); return r; }
DI float max2(float a, float b) { float r; asm("v_max_f32 %0, %1, %2" : "=v"(r) : "v"(a), "v"(b)); return r; }
DI float blo(unsigned u) { return asf(u << 16); }
DI float bhi(unsigned u) { return asf(u & 0xffff0000u); }
DI float swadd32(float a, float b) { auto r = __builtin_amdgcn_permlane32_swap(asu(a), asu(b), false, false); unsigned r0 = r[0], r1 = r[1]; return asf(r0) + asf(r1); }
DI float swadd16(float a, float b) { auto r = __builtin_amdgcn_permlane16_swap(asu(a), asu(b), false, false); unsigned r0 = r[0], r1 = r[1]; return asf(r0) + asf(r1); }
template <int C> DI float dppadd(float v) { return v + asf(__builtin_amdgcn_update_dpp(0u, asu(v), C, 0xf, 0xf, true)); }
DI float rsum4(float v) { v = swadd16(v, v); return swadd32(v, v); }
DI float wsum(float v) { v = dppadd<0xB1>(v); v = dppadd<0x4E>(v); v = dppadd<0x141>(v); v = dppadd<0x128>(v); return rsum4(v); }
DI float rmax4(float v) {
  auto a = __builtin_amdgcn_permlane16_swap(asu(v), asu(v), false, false); unsigned a0 = a[0], a1 = a[1]; v = max2(asf(a0), asf(a1));
  auto b = __builtin_amdgcn_permlane32_swap(asu(v), asu(v), false, false); unsigned b0 = b[0], b1 = b[1]; return max2(asf(b0), asf(b1));
}
DI u32x4 halffrag(u32x2 a, u32x2 b) {
  auto p = __builtin_amdgcn_permlane32_swap(a.x, b.x, false, false);
  auto q = __builtin_amdgcn_permlane32_swap(a.y, b.y, false, false);
  unsigned p0 = p[0], p1 = p[1], q0 = q[0], q1 = q[1];
  return (u32x4){p0, q0, p1, q1};
}
DI void poll(const char* addr, u64 tag) {
#pragma unroll 1
  for (int i = 0; i < SPIN_CAP; ++i) {
    u32x2 v; asm volatile("global_load_dwordx2 %0, %1, off sc1\n\ts_waitcnt vmcnt(0)" : "=&v"(v) : "v"(addr) : "memory");
    const unsigned lo = v.x, hi = v.y;
    if (__all(lo == (unsigned)tag && hi == (unsigned)(tag >> 32))) break;
    __builtin_amdgcn_s_sleep(1);
  }
}
DI void put_flag(char* addr, u64 tag) { __hip_atomic_store((GAS u64*)addr, tag, __ATOMIC_RELAXED, __HIP_MEMORY_SCOPE_AGENT); }
DI float granule(u32x2 g, const char* src, unsigned tag) {
  unsigned val = g.x, tg = g.y;
  if (!__all(tg == tag)) {
#pragma unroll 1
    for (int i = 0; i < SPIN_CAP; ++i) {
      u32x2 v; asm volatile("global_load_dwordx2 %0, %1, off sc1\n\ts_waitcnt vmcnt(0)" : "=&v"(v) : "v"(src) : "memory");
      val = v.x; tg = v.y;
      if (__all(tg == tag)) break;
      __builtin_amdgcn_s_sleep(1);
    }
  }
  return asf(val);
}

struct Stamps {};
#define stamp(slot) do {} while (0)
#define cstamp(f) do {} while (0)
#define sstamp(j) do {} while (0)

struct Job {
  rsrc_t rc; unsigned cv, cstride;
  u32x4 c[4], xa; unsigned xb;
};
DI void job_setup(Job& J, const Params& p, int b, int w, int lane) {
  const int grp = b / 3, th = b - 3 * grp;
  const bool q = grp < 30, one = grp < 57;
  J.cstride = q ? 3840u : 3456u;
  J.rc = mk(sel(q, p.wqkv, sel(one, p.w1, p.w3)), grp < 84 ? 640u * J.cstride : 0u);
  J.cv = (unsigned)(8 * (3 * w + th) + (lane >> 3)) * J.cstride + (unsigned)((q ? grp : one ? grp - 30 : grp - 57) * 128 + (lane & 7) * 16);
}
template <int I> DI void job_loadc(Job& J) { J.c[I] = hload4(J.rc, J.cv, I * 192u * J.cstride); }
DI void job_loadx(Job& J, rsrc_t rx, int b, int w, int lane) {
  const int v = 8 * b + w; const unsigned so = (unsigned)(2 * (v & 1023) + (v >> 10)) * 1280u;
  J.xa = hload4(rx, lane * 16u, so); J.xb = hload1(rx, 1024u + lane * 4u, so);
}
DI void job_cols(Job& J, unsigned part  , int w, int lane) {
  land(J.c[0]); land(J.c[1]); land(J.c[2]); land(J.c[3]);
  const int t = lane & 15; const unsigned one = (t & 1) ? 0x3f800000u : 0x00003f80u;
  const bf16x8 sel8 = asb((u32x4){(t >> 1) == 0 ? one : 0u, (t >> 1) == 1 ? one : 0u, (t >> 1) == 2 ? one : 0u, (t >> 1) == 3 ? one : 0u});
  f32x4 d = {0.f, 0.f, 0.f, 0.f};
#pragma unroll
  for (int i = 0; i < 4; ++i) d = mfma(sel8, asb(J.c[i]), d);
  const f32x4 y = {dppadd<0x128>(d.x), dppadd<0x128>(d.y), dppadd<0x128>(d.z), dppadd<0x128>(d.w)};
  if (lane < 32 && !(lane & 8)) *(LDSP f32x4*)(size_t)(part + w * 256u + (lane & 7) * 32u + (lane >> 4) * 16u) = y;
}
DI float dot2(unsigned a, unsigned b, float c) { return __builtin_amdgcn_fdot2_f32_bf16(__builtin_bit_cast(bf16x2, a), __builtin_bit_cast(bf16x2, b), c, false); }
DI void job_rows(Job& J, rsrc_t rctl, int b, int w, int lane, unsigned tag) {
  land(J.xa); land(J.xb);
  const unsigned a0 = J.xa.x, a1 = J.xa.y, a2 = J.xa.z, a3 = J.xa.w, a4 = J.xb, o = 0x3f803f80u;
  const float s = dot2(a4, o, dot2(a3, o, dot2(a2, o, dot2(a1, o, dot2(a0, o, 0.f)))));
  const float q = dot2(a4, a4, dot2(a3, a3, dot2(a2, a2, dot2(a1, a1, dot2(a0, a0, 0.f)))));
  float u = swadd32(s, q);
  u = dppadd<0xB1>(u); u = dppadd<0x4E>(u); u = dppadd<0x141>(u); u = dppadd<0x128>(u); u = swadd16(u, u);
  const float m = asf(__builtin_amdgcn_readlane(asu(u), 0)) * (1.f / 640.f), e2 = asf(__builtin_amdgcn_readlane(asu(u), 32)) * (1.f / 640.f);
  const float rs = __builtin_amdgcn_rsqf(fmaxf(e2 - m * m, 0.f) + 1e-5f);
  __builtin_amdgcn_raw_buffer_store_b64((u32x2){asu(lane ? rs : m), tag}, rctl, lane < 2 ? lane * 8u : OOB, WS_GR + (8 * b + w) * 16u, SC1);
}
DI void copy_wflags(rsrc_t rctl, unsigned dst, int half, int lane) { dma_dev(rctl, (half == 0 || lane < 10) ? lane * 128u : OOB, WS_FW + half * 8192u, dst + half * 1024u); }
DI void check_wflags(const Params& p, unsigned src, int lane, u64 tag) {
  const u32x2 f = ld64(src + lane * 16u), e = ld64(src + (lane < 10 ? 1024u + lane * 16u : 0u));
  const unsigned f0 = f.x, f1 = f.y, e0 = e.x, e1 = e.y, lo = (unsigned)tag, hi = (unsigned)(tag >> 32);
  if (!__all(f0 == lo && f1 == hi && e0 == lo && e1 == hi)) { poll(p.ws + WS_FW + lane * 128u, tag); poll(p.ws + WS_FW + (lane < 10 ? 64 + lane : 0) * 128u, tag); }
}
DI void job_pubc(unsigned part, rsrc_t rctl, int b, int lane, unsigned tag) {
  float s = 0.f;
#pragma unroll
  for (int k = 0; k < 8; ++k) s += ld32f(part + k * 256u + lane * 4u);
  const int grp = b / 3, th = b - 3 * grp;
  __builtin_amdgcn_raw_buffer_store_b64((u32x2){asu(s), tag}, rctl, grp < 84 ? lane * 8u : OOB, WS_GC + (unsigned)(th * 5376 + grp * 64) * 8u, SC1);
}

struct P2 { rsrc_t rq; unsigned q0, q2, qb, lv; bool last; };
DI void p2_setup(P2& Q, const Params& p, int n, int w, int lane) {
  Q.rq = mk(p.ws + WS_W, W_BYTES);
  const int base = w < 4 ? 17 + 6 * w : w < 6 ? 10 + 8 * w : 16 + 7 * w;
  Q.q0 = w; Q.q2 = w < 4 ? 16 + w : base + 2; Q.qb = base; Q.last = (w == 4 || w == 5); Q.lv = lane * 16u;
}
template <int SL> DI unsigned p2_step(const P2& Q) { return SL == 0 ? Q.q0 : SL == 1 ? Q.q0 + 8 : SL == 2 ? Q.q2 : Q.qb + SL; }
template <int F> DI unsigned p2_wofs(const P2& Q, int n) {
  const unsigned o = (n * 74u + p2_step<F / 5>(Q)) * 5120u + (F % 5) * 1024u;
  return (F / 5 == 9 && !Q.last) ? OOB : o;
}

struct Ops { int n; int v[400]; int mark[22]; };
constexpr int waitN(const Ops& o, int j) {
  const int end = o.mark[j + 1];
  int last = -1;
  for (int i = 0; i < end; ++i) if (o.v[i] == 100 + j + 1 || ((j + 1) % 2 == 0 && o.v[i] == 200 + (j + 1) / 2)) last = i;
  const int n = last < 0 ? 63 : end - 1 - last;
  return n > 63 ? 63 : n;
}
constexpr int dlist(int NW, int j, int k) {
  int L[16] = {}; int n = 0;
  auto W = [&](int s) { for (int i = 0; i < NW; ++i) L[n++] = 1000 + s * 4 + i; };
  auto X = [&](int s, int h) { L[n++] = 300 + 2 * s + h; };
  if (j == -1) { W(0); X(0, 0); X(0, 1); W(1); }
  else if (j == 0) { W(2); X(1, 0); X(1, 1); W(3); }
  else if (j == 1) { X(2, 0); X(2, 1); W(4); W(5); X(3, 0); }
  else { W(j + 4); if (j % 2 == 0) X(j / 2 + 2, 1); else X((j - 1) / 2 + 3, 0); }
  return k < n ? L[k] : -1;
}
constexpr Ops make_ops(int NW, bool tail, int xrep = 1) {
  Ops o{}; int n = 0;
  for (int j = -1; j < 20; ++j) {
    o.mark[j + 1] = n;
    for (int k = 0; k < 16; ++k) {
      const int c = dlist(NW, j, k); if (c < 0) break;
      const int s = c >= 1000 ? (c - 1000) / 4 : (c - 300) / 2;
      const bool real = c >= 1000 ? s < 20 : s < 10;
      if (real) { if (c >= 1000) o.v[n++] = 100 + s; else for (int r = 0; r < xrep; ++r) o.v[n++] = 200 + s; } else if (tail) o.v[n++] = 0;
    }
    if (j == -1) o.mark[0] = n;
  }
  o.n = n; return o;
}
constexpr Ops F_OPS = make_ops(3, true), A_OPS = make_ops(2, false), A_OPS7 = make_ops(1, false, 2);
constexpr int waitA(int j) { return waitN(A_OPS, j) < waitN(A_OPS7, j) ? waitN(A_OPS, j) : waitN(A_OPS7, j); }

constexpr unsigned F_WS = 24576, F_XR = 98304, F_XS = 16384, F_EX = 147456;
constexpr unsigned FX_G = F_EX, FX_CS = F_EX + 11264, FX_ST = FX_CS + 1536, FX_FW = FX_ST + 1024, FX_JP = F_EX;
DI void ffn_tile(const Params& p, Stamps& ST, Job& J, const P2& Q, unsigned lds, int b, int w, int lane, int par, int rtl, int ct, int n2, int myflag, u64& tag) {
  const int mg = w >> 2, ng = w & 3;
  const int t = lane & 15, g = lane >> 4;
  const rsrc_t rx = mk(p.x, 2048u * 1280u), r1 = mk(p.w1, 640u * 3456u), r3 = mk(p.w3, 640u * 3456u);
  const rsrc_t rm = mk(sel(mg, p.w3, p.w1), 640u * 3456u), rctl = mk(p.ws, CTL_BYTES);
  const unsigned wv = (unsigned)(8 * ng + (lane >> 3)) * 3456u + (unsigned)((((lane & 7) >> 1) ^ (((lane >> 4) & 1) | ((ng & 1) << 1))) * 32 + (lane & 1) * 16);
  const unsigned ws0 = ct * 384u + mg * 128u, ws1 = ct * 384u + (mg ? 0u : 256u), ws2 = ct * 384u + (mg + 1) * 128u;
  const unsigned xv = (unsigned)(2 * (8 * w + (lane >> 3))) * 1280u + (unsigned)(((lane & 7) ^ (((lane >> 4) & 3) | ((w & 1) << 2))) * 16);
  const unsigned xs0 = (unsigned)(2 * 128 * rtl + par) * 1280u;
  const unsigned lw = lds + w * 1024u;
  auto issue = [&](auto C) {
    constexpr int c = C.value;
    if constexpr (c >= 1000) {
      constexpr int s = (c - 1000) / 4, i = (c - 1000) % 4, row = (s & 3) * 3 + i;
      if constexpr (s < 20) dma(i == 0 ? r1 : i == 1 ? rm : r3, wv, s * (32u * 3456u) + (i == 0 ? ws0 : i == 1 ? ws1 : ws2), lw + row * 8192u);
      else dma_dev(Q.rq, Q.lv, p2_wofs<32 + row>(Q, n2), lw + row * 8192u);
    } else if constexpr (c >= 0) {
      constexpr int s = (c - 300) / 2, h = (c - 300) % 2, row = 12 + (s % 3) * 2 + h;
      if constexpr (s < 10) dma(rx, xv, xs0 + s * 128u + h * (128u * 1280u), lw + row * 8192u);
      else dma_dev(Q.rq, Q.lv, p2_wofs<32 + row>(Q, n2), lw + row * 8192u);
    }
  };
  sstamp(31);
  job_loadc<0>(J); job_loadx(J, rx, b, w, lane);
  sfor<8>([&](auto K) { issue(IC<dlist(3, -1, K.value)>{}); });
  sstamp(32);
  unsigned xa[2], wa[2][3];
  xa[0] = lds + F_XR + (unsigned)(64 * mg + t) * 128u + (unsigned)((g ^ (t >> 1)) * 16); xa[1] = opaque(xa[0] ^ 64u);
  {
    const int q = t >> 2, pp = t & 3, f = (q >> 1) | ((g & 1) << 1);
#pragma unroll
    for (int i = 0; i < 3; ++i) { const int fb = 3 * ng + i; wa[0][i] = lds + (fb >> 2) * 4096u + (8 * g + q) * 128u + (((fb & 3) ^ f) * 32) + 8 * pp; wa[1][i] = opaque(wa[0][i] + 2 * F_WS); }
  }
  f32x4 acc[6][4];
#pragma unroll
  for (int a = 0; a < 6; ++a)
#pragma unroll
    for (int c = 0; c < 4; ++c) acc[a][c] = (f32x4){0.f, 0.f, 0.f, 0.f};
  bf16x8 X[2][4]; u32x2 W[2][12];
  auto rd = [&](auto S, auto R) {
    constexpr int s = S.value, r = R.value;
    if constexpr (r < 4) X[s & 1][r] = ld128(xa[s & 1] + ((s >> 1) % 3) * F_XS + r * 2048u);
    else { constexpr int k = r - 4, bb = k >> 1; W[s & 1][k] = tr64(wa[(s >> 1) & 1][bb % 3] + (s & 1) * F_WS + (bb / 3) * 12288u + (k & 1) * 512u); }
  };
  tag = first_bar<waitN(F_OPS, -1)>(p.ws) + 1;
  stamp(1); cstamp(c0); sstamp(33);
  sfor<16>([&](auto R) { rd(IC<0>{}, R); });
  sfor<20>([&](auto JC) {
    constexpr int j = JC.value;
    wait_bar<j == 19 ? 8 : waitN(F_OPS, j)>();
    sstamp(j);
    if constexpr (j == 15) check_wflags(p, lds + FX_FW, lane, tag);
    if constexpr (j == 9) { if (w == 1) job_pubc(lds + FX_JP, rctl, b, lane, (unsigned)tag); }
    sfor<24>([&](auto MC) {
      constexpr int m = MC.value, bb = m / 4, a = m % 4;
      acc[bb][a] = mfma(cat(W[j & 1][2 * bb], W[j & 1][2 * bb + 1]), X[j & 1][a], acc[bb][a]);
      if constexpr (j < 19 && m < 16 && !(ABL & 4)) rd(IC<j + 1>{}, MC);
      if constexpr (!(ABL & 2)) {
        if constexpr (j < 2) { if constexpr (m >= 6 && m % 2 == 0) issue(IC<dlist(3, j, (m - 6) / 2)>{}); }
        else if constexpr (m >= 16 && m % 2 == 0) issue(IC<dlist(3, j, (m - 16) / 2)>{});
      }
      if constexpr (m == 23) {
        if constexpr (j == 0) job_loadc<1>(J);
        if constexpr (j == 2) job_loadc<2>(J);
        if constexpr (j == 4) job_loadc<3>(J);
        if constexpr (j == 12) { if (w < 2) copy_wflags(rctl, lds + FX_FW, w, lane); }
        if constexpr (j == 16) {
          if (w < 6) {
            const unsigned so = WS_GC + (unsigned)((w % 3) * 5376 + 1920 + (w / 3) * 1728 + 192 * ct) * 8u;
            dma_dev(rctl, Q.lv, so, lds + FX_G + w * 1536u);
            if (lane < 32) dma_dev(rctl, Q.lv, so + 1024u, lds + FX_G + w * 1536u + 1024u);
          } else dma_dev(rctl, Q.lv, WS_GR + (unsigned)(par * 1024 + rtl * 128 + (w - 6) * 64) * 16u, lds + FX_G + 9216u + (w - 6) * 1024u);
        }
        if constexpr (j == 19) dma_dev(Q.rq, Q.lv, p2_wofs<32 + 13>(Q, n2), lw + 13 * 8192u);
      }
      if constexpr (j == 6 && m == 3) job_rows(J, rctl, b, w, lane, (unsigned)tag);
      if constexpr (j == 8 && m == 3) job_cols(J, lds + FX_JP, w, lane);
      if constexpr (!((j == 6 || j == 8) && m >= 3 && m < 12)) GAP();
    });
  });
  stamp(2); cstamp(c1); sstamp(20);
  {
    const int tid = w * 64 + lane;
    if (w < 6) {
      const int up = tid >= 192, c = tid - 192 * up;
      const char* src = p.ws + WS_GC + (1920 + up * 1728 + 192 * ct + c) * 8u;
      const unsigned a = lds + FX_G + up * 4608u + c * 8u;
      const float s = granule(ld64(a), src, (unsigned)tag) + granule(ld64(a + 1536u), src + 5376 * 8, (unsigned)tag) + granule(ld64(a + 3072u), src + 2 * 5376 * 8, (unsigned)tag);
      *(LDSP float*)(size_t)(lds + FX_CS + tid * 4u) = s;
    } else {
      const int r = tid - 384;
      const char* src = p.ws + WS_GR + (par * 1024 + rtl * 128 + r) * 16u;
      const u32x4 v = ld128u(lds + FX_G + 9216u + r * 16u);
      const float m = granule((u32x2){v.x, v.y}, src, (unsigned)tag), rs = granule((u32x2){v.z, v.w}, src + 8, (unsigned)tag);
      *(LDSP f32x2*)(size_t)(lds + FX_ST + r * 8u) = (f32x2){m, rs};
    }
  }
  sstamp(21);
  lds_bar();
  sstamp(22);
  f32x4 cG[3], cU[3];
#pragma unroll
  for (int i = 0; i < 3; ++i) { cG[i] = ld128f(lds + FX_CS + ((3 * ng + i) * 16 + 4 * g) * 4u); cU[i] = ld128f(lds + FX_CS + 768u + ((3 * ng + i) * 16 + 4 * g) * 4u); }
  const rsrc_t rh = mk(p.ws + WS_H, H_BYTES);
  const unsigned hv = (unsigned)(lane >> 5) * 1024u + (lane & 31) * 16u;
  const unsigned hs = (unsigned)(2 * (8 * par + rtl) + mg) * (74u * 4096u);
#pragma unroll
  for (int ap = 0; ap < 2; ++ap) {
    u32x2 P[2][3];
#pragma unroll
    for (int a2 = 0; a2 < 2; ++a2) {
      const int a = 2 * ap + a2;
      const f32x2 st = ld64f(lds + FX_ST + (64 * mg + 16 * a + t) * 8u);
      const f32x2 nm = bc2(-st.x), k1 = bc2(st.y * -1.44269504089f), ik = bc2(__builtin_amdgcn_rcpf(st.y * st.y));
      f32x2 G[6], U[6], E[6];
#pragma unroll
      for (int i = 0; i < 3; ++i) {
        G[2 * i] = fma2(nm, lo2(cG[i]), lo2(acc[i][a])); G[2 * i + 1] = fma2(nm, hi2(cG[i]), hi2(acc[i][a]));
        U[2 * i] = fma2(nm, lo2(cU[i]), lo2(acc[3 + i][a])); U[2 * i + 1] = fma2(nm, hi2(cU[i]), hi2(acc[3 + i][a]));
      }
#pragma unroll
      for (int k = 0; k < 6; ++k) E[k] = G[k] * k1;
#pragma unroll
      for (int k = 0; k < 6; ++k) { E[k] = (f32x2){__builtin_amdgcn_exp2f(E[k].x), __builtin_amdgcn_exp2f(E[k].y)}; G[k] = G[k] * U[k]; }
#pragma unroll
      for (int k = 0; k < 6; ++k) E[k] = fma2(E[k], ik, ik);
#pragma unroll
      for (int k = 0; k < 6; ++k) E[k] = (f32x2){__builtin_amdgcn_rcpf(E[k].x), __builtin_amdgcn_rcpf(E[k].y)};
#pragma unroll
      for (int i = 0; i < 3; ++i) P[a2][i] = (u32x2){pk2(G[2 * i] * E[2 * i]), pk2(G[2 * i + 1] * E[2 * i + 1])};
      GAP();
    }
#pragma unroll
    for (int i = 0; i < 3; ++i) {
      const int fb = 12 * ct + 3 * ng + i;
      __builtin_amdgcn_raw_buffer_store_b128(halffrag(P[0][i], P[1][i]), rh, hv, hs + (20 + (fb >> 1)) * 4096u + ap * 2048u + (fb & 1) * 512u, SC1);
    }
  }
  sstamp(23);
  wait_bar<0>();
  sstamp(24); sstamp(25); sstamp(26); sstamp(27); sstamp(28);
  if (w == 0 && lane == 0) put_flag(p.ws + WS_FH + myflag * 64u, tag);
}

constexpr unsigned A_WS = 15360, A_XR = 61440, A_XS = 20480, A_JOB = 122880;
constexpr unsigned AX_G = A_JOB, AX_CS = A_JOB + 10240, AX_ST = AX_CS + 1024, AX_FW = AX_ST + 1536, AX_JP = AX_FW + 2048;
DI void att_tile(const Params& p, Stamps& ST, Job& J, const P2& Q, unsigned lds, int b, int w, int lane, int par, int jt, int hg, int job, int n2, int myflag, u64& tag) {
  const int mg = w >> 2, h = (w + mg) & 3;
  const int t = lane & 15, g = lane >> 4;
  const rsrc_t rx = mk(p.x, 2048u * 1280u), rw = mk(p.wqkv, 640u * 3840u), rctl = mk(p.ws, CTL_BYTES);
  unsigned wv[2];
#pragma unroll
  for (int i = 0; i < 2; ++i) {
    const int n = (2 * w + i) * 64 + lane, pos = n / 30, cc = n - pos * 30, sec = cc / 10;
    const int k = (pos & 3) | ((pos & 4) << 1) | ((pos & 8) >> 1) | (pos & 16);
    wv[i] = (unsigned)(k * 3840 + sec * 1280 + (cc - sec * 10) * 16);
  }
  const unsigned ws0 = hg * 160u;
  const unsigned xv = (unsigned)(2 * (8 * w + (lane >> 3))) * 1280u + (unsigned)(((lane & 7) ^ (((lane >> 4) & 3) | ((w & 1) << 2))) * 16);
  const unsigned xs0 = (unsigned)(2 * 144 * jt + par) * 1280u;
  auto issue = [&](auto C) {
    constexpr int c = C.value;
    if constexpr (c >= 1000) {
      constexpr int s = (c - 1000) / 4, i = (c - 1000) % 4;
      if constexpr (s < 20) { if (i == 0 || w < 7) dma(rw, wv[i], s * (32u * 3840u) + ws0, lds + w * 2048u + (s & 3) * A_WS + i * 1024u); }
    } else if constexpr (c >= 0) {
      constexpr int s = (c - 300) / 2, hf = (c - 300) % 2;
      if constexpr (s < 10) {
        const unsigned so = xs0 + s * 128u + hf * (160u * 1280u), l = lds + A_XR + w * 1024u + (s % 3) * A_XS + hf * 10240u;
        dma(rx, xv, so, l);
        if (w >= 6) dma(rx, xv, so + 32u * 1280u, l + 2048u);
      }
    }
  };
  sstamp(31);
  job_loadc<0>(J); job_loadx(J, rx, b, w, lane);
  sfor<6>([&](auto K) { issue(IC<dlist(2, -1, K.value)>{}); });
  sstamp(32);
  unsigned xa[2], wm, wt;
  xa[0] = lds + A_XR + (unsigned)(80 * mg + t) * 128u + (unsigned)((g ^ (t >> 1)) * 16); xa[1] = opaque(xa[0] ^ 64u);
  {
    const int q = t >> 2, pp = t & 3;
    const unsigned rowb = lds + (unsigned)(q + 4 * (g & 1) + 16 * (g >> 1)) * 480u;
    wm = rowb + 40 * h + 8 * pp; wt = rowb + 40 * pp + 32 + h * 160;
  }
  f32x4 acc[4][5];
#pragma unroll
  for (int a = 0; a < 4; ++a)
#pragma unroll
    for (int c = 0; c < 5; ++c) acc[a][c] = (f32x4){0.f, 0.f, 0.f, 0.f};
  bf16x8 X[2][5]; u32x2 W[2][8];
  auto rd = [&](auto S, auto R) {
    constexpr int s = S.value, r = R.value;
    if constexpr (r < 5) X[s & 1][r] = ld128(xa[s & 1] + ((s >> 1) % 3) * A_XS + r * 2048u);
    else { constexpr int k = r - 5; W[s & 1][k] = tr64((k < 6 ? wm + (k >> 1) * 160u : wt) + (s & 3) * A_WS + (k & 1) * 3840u); }
  };
  W[0][6] = W[0][7] = W[1][6] = W[1][7] = (u32x2){0u, 0u};
  tag = first_bar<waitA(-1)>(p.ws) + 1;
  stamp(1); cstamp(c0); sstamp(33);
  sfor<11>([&](auto R) { rd(IC<0>{}, R); });
  if (h < 3) { rd(IC<0>{}, IC<11>{}); rd(IC<0>{}, IC<12>{}); }
  const bool isO = job < 20;
  sfor<20>([&](auto JC) {
    constexpr int j = JC.value;
    wait_bar<j == 8 ? 6 : waitA(j)>();
    sstamp(j);
    if constexpr (j == 9) { if (w == 1) job_pubc(lds + AX_JP, rctl, b, lane, (unsigned)tag); }
    if constexpr (j == 8) { if (job < 74 && w == 0 && lane == 0) put_flag(p.ws + WS_FW + job * 128u, tag); }
    sfor<15>([&](auto MC) {
      constexpr int m = MC.value, bb = m / 5, a = m % 5;
      acc[bb][a] = mfma(cat(W[j & 1][2 * bb], W[j & 1][2 * bb + 1]), X[j & 1][a], acc[bb][a]);
      if constexpr (j < 19 && m < 11) rd(IC<j + 1>{}, MC);
      if constexpr (j < 2) { if constexpr (m >= 4 && m < 11) issue(IC<dlist(2, j, m - 4)>{}); }
      else if constexpr (m >= 11 && m < 14) issue(IC<dlist(2, j, m - 11)>{});
      if constexpr (j == 6 && m == 3) job_rows(J, rctl, b, w, lane, (unsigned)tag);
      if constexpr (j == 8 && m == 3) job_cols(J, lds + AX_JP, w, lane);
      if constexpr (!((j == 6 || j == 8) && m >= 3 && m < 11)) GAP();
    });
    if (h < 3) {
      if constexpr (j < 19) { rd(IC<j + 1>{}, IC<11>{}); rd(IC<j + 1>{}, IC<12>{}); }
#pragma unroll
      for (int a = 0; a < 5; ++a) acc[3][a] = mfma(cat(W[j & 1][6], W[j & 1][7]), X[j & 1][a], acc[3][a]);
    }
    if constexpr (j == 0) job_loadc<1>(J);
    if constexpr (j == 2) job_loadc<2>(J);
    if constexpr (j == 4) job_loadc<3>(J);
    if constexpr (j == 1) {
      if (job < 74) {
        const rsrc_t rj = mk(sel(isO, p.wo, p.w2), isO ? 640u * 1280u : 1728u * 1280u);
        const int db = 2 * job + (w >> 2), q5 = db / 5, e = db - 5 * q5;
        const int u = !isO ? 32 * (job - 20) + 4 * w : e < 4 ? 20 * (4 * q5 + e) + 4 * (w & 3) : 20 * (4 * q5 + (w & 3)) + 16;
#pragma unroll
        for (int i = 0; i < 5; ++i) dma(rj, g * 1280u + t * 16u, u * 1280u + i * 256u, lds + A_JOB + (5 * w + i) * 1024u);
      }
    }
    if constexpr (j == 5) {
      if (job < 74) {
        const unsigned a = lds + A_JOB + (unsigned)(4 * (g >> 1) + (g & 1)) * 5120u + (t >> 2) * 256u + (t & 3) * 8u;
#pragma unroll
        for (int cb = 0; cb < 5; ++cb) {
          const unsigned cbyte = 160u * w + 32u * cb, o = (cbyte >> 8) * 1024u + (cbyte & 255u);
          const u32x2 lo = tr64(a + o), hi = tr64(a + o + 2 * 5120u);
          __builtin_amdgcn_raw_buffer_store_b128((u32x4){lo.x, lo.y, hi.x, hi.y}, Q.rq, Q.lv, (unsigned)(w * 74 + job) * 5120u + cb * 1024u, SC1);
        }
      }
    }
    if constexpr (j == 16) {
      if (lane < 40) {
        dma_dev(rctl, Q.lv, WS_GC + (unsigned)((w % 3) * 5376 + (w / 3) * 640 + 80 * hg) * 8u, lds + AX_G + w * 640u);
        if (w == 0) dma_dev(rctl, Q.lv, WS_GC + (unsigned)(2 * 5376 + 2 * 640 + 80 * hg) * 8u, lds + AX_G + 8 * 640u);
      }
      if (w >= 1 && w < 4) dma_dev(rctl, (w < 3 || lane < 32) ? Q.lv : OOB, WS_GR + (unsigned)(par * 1024 + jt * 144 + (w - 1) * 64) * 16u, lds + AX_G + 6144u + (w - 1) * 1024u);
      if (w >= 4 && w < 6) copy_wflags(rctl, lds + AX_FW, w - 4, lane);
    }
    GAP();
  });
  stamp(2); cstamp(c1); sstamp(20);
  wait_bar<0>();
  sstamp(21);
  {
    const int tid = w * 64 + lane;
    if (w < 4) {
      const int c = tid < 240 ? tid : 0, sec = c / 80, cc = c - 80 * sec;
      const char* src = p.ws + WS_GC + (sec * 640 + 80 * hg + cc) * 8u;
      const unsigned a = lds + AX_G + sec * 1920u + cc * 8u;
      const float s = granule(ld64(a), src, (unsigned)tag) + granule(ld64(a + 640u), src + 5376 * 8, (unsigned)tag) + granule(ld64(a + 1280u), src + 2 * 5376 * 8, (unsigned)tag);
      *(LDSP float*)(size_t)(lds + AX_CS + tid * 4u) = s;
    } else if (w < 7) {
      const int r0 = tid - 256, r = r0 < 160 ? r0 : 0;
      const char* src = p.ws + WS_GR + (par * 1024 + jt * 144 + r) * 16u;
      const u32x4 v = ld128u(lds + AX_G + 6144u + r * 16u);
      const float m = granule((u32x2){v.x, v.y}, src, (unsigned)tag), rs = granule((u32x2){v.z, v.w}, src + 8, (unsigned)tag);
      *(LDSP f32x2*)(size_t)(lds + AX_ST + r0 * 8u) = (f32x2){m, rs};
    }
    check_wflags(p, lds + AX_FW, lane, tag);
  }
  sstamp(22);
  lds_bar();
  sstamp(23);
  constexpr unsigned E_KB = 0, E_X = 2048, E_OT = E_X + 40960;
  u32x2 Qm[5], K[6];
  {
    f32x4 c4[4];
#pragma unroll
    for (int bb = 0; bb < 3; ++bb) c4[bb] = ld128f(lds + AX_CS + (bb * 80 + 20 * h + 4 * g) * 4u);
    c4[3] = ld128f(lds + AX_CS + ((h < 3 ? h : 0) * 80 + 20 * g + 16) * 4u);
    const unsigned xw = lds + E_X + (unsigned)(5 * mg) * 4096u + t * 32u + g * 8u;
#pragma unroll
    for (int a = 0; a < 5; ++a) {
      const f32x2 st = ld64f(lds + AX_ST + (80 * mg + 16 * a + t) * 8u);
      u32x2 PK[4];
      const f32x2 nm = bc2(-st.x), rs = bc2(st.y);
#pragma unroll
      for (int bb = 0; bb < 4; ++bb) PK[bb] = (u32x2){pk2(fma2(nm, lo2(c4[bb]), lo2(acc[bb][a])) * rs), pk2(fma2(nm, hi2(c4[bb]), hi2(acc[bb][a])) * rs)};
      Qm[a] = PK[0]; K[a + 1] = PK[1];
      *(LDSP u32x2*)(size_t)(xw + a * 4096u + h * 512u) = PK[2];
      *(LDSP u32x2*)(size_t)(xw + a * 4096u + (4 + h) * 512u) = PK[3];
    }
    if (mg == 0) *(LDSP u32x2*)(size_t)(lds + E_KB + h * 512u + lane * 8u) = K[5];
  }
  sstamp(24);
  lds_bar();
  sstamp(25);
  const rsrc_t rh = mk(p.ws + WS_H, H_BYTES);
  const int B0 = par * 64 + 9 * jt;
  {
    const unsigned xr = lds + E_X + t * 32u + g * 8u;
    const unsigned xt = lds + E_X + (unsigned)(4 * g + (t >> 2)) * 32u + (t & 3) * 8u;
    const int pb = mg ? 4 : 0;
    u32x2 Tq[5], Tk[6], V[6], Tv[6];
    K[0] = ld64(lds + E_KB + h * 512u + lane * 8u);
    Tk[0] = ld64(xr + pb * 4096u + 5 * 512u);
#pragma unroll
    for (int a = 0; a < 5; ++a) { const unsigned o = (unsigned)(5 * mg + a) * 4096u; Tq[a] = ld64(xr + o + 4 * 512u); Tk[a + 1] = ld64(xr + o + 5 * 512u); }
    V[0] = tr64(xt + pb * 4096u + h * 512u); Tv[0] = tr64(xt + pb * 4096u + 6 * 512u);
#pragma unroll
    for (int a = 0; a < 5; ++a) { const unsigned o = (unsigned)(5 * mg + a) * 4096u; V[a + 1] = tr64(xt + o + h * 512u); Tv[a + 1] = tr64(xt + o + 6 * 512u); }
    if (mg == 0) K[0] = K[1];
    bool vp[4]; float bias[4], bias0[4];
#pragma unroll
    for (int r = 0; r < 4; ++r) { const int kk = 4 * g + r; vp[r] = kk >= t + 9; const bool vc = kk <= t && kk >= t - 7;
      bias[r] = (vp[r] || vc) ? 0.f : -INFINITY; bias0[r] = ((vp[r] && mg) || vc) ? 0.f : -INFINITY; }
    const unsigned mp0 = (vp[0] ? 0xffffu : 0u) | (vp[1] ? 0xffff0000u : 0u), mp1 = (vp[2] ? 0xffffu : 0u) | (vp[3] ? 0xffff0000u : 0u);
    const unsigned qm = g == h ? 0xffffffffu : 0u;
    const float c = 0.22360679775f * 1.44269504089f;
    const f32x4 z = {0.f, 0.f, 0.f, 0.f};
    f32x4 sp[5], sc[5];
#pragma unroll
    for (int a = 0; a < 5; ++a) {
      const bf16x8 qf = cat(Qm[a], (u32x2){Tq[a].x & qm, Tq[a].y & qm});
      sp[a] = mfma(cat(K[a], Tk[a]), qf, z); sc[a] = mfma(cat(K[a + 1], Tk[a + 1]), qf, z);
    }
    GAP();
    float s[5][4], mx[5], il[5];
#pragma unroll
    for (int a = 0; a < 5; ++a) {
#pragma unroll
      for (int r = 0; r < 4; ++r) s[a][r] = fmaf(vp[r] ? sp[a][r] : sc[a][r], c, a == 0 ? bias0[r] : bias[r]);
      mx[a] = max2(max3(s[a][0], s[a][1], s[a][2]), s[a][3]);
    }
#pragma unroll
    for (int a = 0; a < 5; ++a) mx[a] = rmax4(mx[a]);
    GAP();
    f32x4 om[5], ot[5];
#pragma unroll
    for (int a = 0; a < 5; ++a) {
#pragma unroll
      for (int r = 0; r < 4; ++r) s[a][r] = __builtin_amdgcn_exp2f(s[a][r] - mx[a]);
      const unsigned p0 = pk(s[a][0], s[a][1]), p1 = pk(s[a][2], s[a][3]);
      const bf16x8 pf = asb((u32x4){p0 & mp0, p1 & mp1, p0 & ~mp0, p1 & ~mp1});
      om[a] = mfma(cat(V[a], V[a + 1]), pf, z); ot[a] = mfma(cat(Tv[a], Tv[a + 1]), pf, z);
      il[a] = (s[a][0] + s[a][1]) + (s[a][2] + s[a][3]);
    }
#pragma unroll
    for (int a = 0; a < 5; ++a) il[a] = __builtin_amdgcn_rcpf(rsum4(il[a]));
    GAP();
    u32x2 O[6];
#pragma unroll
    for (int a = 0; a < 5; ++a) {
      const f32x2 i2 = bc2(il[a]);
      O[a] = (u32x2){pk2(lo2(om[a]) * i2), pk2(hi2(om[a]) * i2)};
      const u32x2 tl = {pk2(lo2(ot[a]) * i2), pk2(hi2(ot[a]) * i2)};
      if (g == h) *(LDSP u32x2*)(size_t)(lds + E_OT + (5 * mg + a) * 512u + ((h & 1) * 16 + t) * 16u + (h >> 1) * 8u) = tl;
    }
    O[5] = O[4];
    const int db = 5 * hg + h;
    const unsigned so = (db >> 1) * 4096u + (db & 1) * 512u;
#pragma unroll
    for (int ap = 0; ap < 3; ++ap) {
      const int lb = 5 * mg + 2 * ap + (lane >> 5), B = B0 + lb;
      const bool ok = (lb > 0 || jt == 0) && (ap < 2 || lane < 32);
      const unsigned v = ok ? (unsigned)(B >> 2) * (74u * 4096u) + (B & 3) * 1024u + (lane & 31) * 16u : OOB;
      __builtin_amdgcn_raw_buffer_store_b128(halffrag(O[2 * ap], O[2 * ap + 1]), rh, v, so, SC1);
    }
  }
  sstamp(26);
  lds_bar();
  {
    const int lb = 2 * w + (lane >> 5), B = B0 + lb, db = 5 * hg + 4;
    const u32x4 d = ld128u(lds + E_OT + (w < 5 ? w : 0) * 1024u + lane * 16u);
    const unsigned v = (w < 5 && (lb > 0 || jt == 0)) ? (unsigned)(B >> 2) * (74u * 4096u) + (B & 3) * 1024u + (lane & 31) * 16u : OOB;
    __builtin_amdgcn_raw_buffer_store_b128(d, rh, v, (db >> 1) * 4096u + (db & 1) * 512u, SC1);
  }
  sstamp(27);
  wait_bar<0>();
  sstamp(28);
  if (w == 0 && lane == 0) put_flag(p.ws + WS_FH + myflag * 64u, tag);
  sfor<18>([&](auto R) { dma_dev(Q.rq, Q.lv, p2_wofs<32 + R.value>(Q, n2), lds + w * 1024u + R.value * 8192u); });
}

DI void out_tile(const Params& p, Stamps& ST, const P2& Q, unsigned lds, int w, int lane, int mt, int n, u64 tag) {
  const rsrc_t rh = mk(p.ws + WS_H, H_BYTES);
  const int t = lane & 15, g = lane >> 4;
  const rsrc_t rx = mk(p.x, 2048u * 1280u), ro = mk(p.out, 2048u * 1280u);
  stamp(3);
  u32x4 Wf[32];
  sfor<32>([&](auto F) { constexpr int f = F.value; Wf[f] = __builtin_amdgcn_raw_buffer_load_b128(Q.rq, Q.lv, p2_wofs<f>(Q, n), SC1); });
  const int par = mt >> 4, m15 = mt & 15;
  if (w == 7) { const int jl = m15 ? (4 * m15 - 1) / 9 : 0, jh = (4 * m15 + 2) / 9;
                poll(p.ws + WS_FH + (144 + (par * 7 + ((lane & 8) ? jh : jl)) * 8 + (lane & 7)) * 64u, tag); }
  lds_bar();
  stamp(4);
  f32x4 acc[5][4];
#pragma unroll
  for (int a = 0; a < 5; ++a)
#pragma unroll
    for (int c = 0; c < 4; ++c) acc[a][c] = (f32x4){0.f, 0.f, 0.f, 0.f};
  u32x4 Hf[2][4];
  auto loadH = [&](auto S) {
    constexpr int s = S.value;
    const unsigned so = (s == 9 && !Q.last) ? OOB : (mt * 74u + p2_step<s>(Q)) * 4096u;
#pragma unroll
    for (int i = 0; i < 4; ++i) Hf[s & 1][i] = __builtin_amdgcn_raw_buffer_load_b128(rh, Q.lv + i * 1024u, so, SC1);
  };
  auto comp = [&](auto S) {
    constexpr int s = S.value;
    sfor<5>([&](auto C) {
      constexpr int cb = C.value, f = s * 5 + cb;
      bf16x8 wf;
      if constexpr (f < 32) wf = asb(Wf[f]); else wf = ld128(lds + w * 1024u + (f - 32) * 8192u + Q.lv);
#pragma unroll
      for (int a = 0; a < 4; ++a) acc[cb][a] = mfma(wf, asb(Hf[s & 1][a]), acc[cb][a]);
    });
  };
  loadH(IC<0>{}); loadH(IC<1>{});
  GAP();
  comp(IC<0>{}); GAP(); comp(IC<1>{}); GAP();
  if (w == 7) poll(p.ws + WS_FH + ((mt >> 1) * 9 + (lane < 9 ? lane : 0)) * 64u, tag);
  lds_bar();
  stamp(5);
  loadH(IC<2>{}); loadH(IC<3>{});
  unsigned ov[2];
  u32x4 xr[2];
#pragma unroll
  for (int i = 0; i < 2; ++i) {
    const int cc = (w + 8 * i) * 4 + g, tb = cc / 10, c8 = cc - tb * 10;
    const int B = 4 * mt + tb, tok = 2 * ((B & 63) * 16 + t) + (B >> 6);
    ov[i] = (i == 0 || w < 2) ? (unsigned)(tok * 1280 + n * 160 + c8 * 16) : OOB;
  }
  GAP();
  sfor<8>([&](auto K) {
    constexpr int s = 2 + K.value;
    comp(IC<s>{}); GAP();
    if constexpr (s + 2 < 10) loadH(IC<s + 2>{});
    if constexpr (s == 7) { xr[0] = __builtin_amdgcn_raw_buffer_load_b128(rx, ov[0], 0, 0); xr[1] = __builtin_amdgcn_raw_buffer_load_b128(rx, ov[1], 0, 0); }
    GAP();
  });
  stamp(6);
  lds_bar();
#pragma unroll
  for (int a = 0; a < 4; ++a)
#pragma unroll
    for (int c = 0; c < 5; ++c) *(LDSP f32x4*)(size_t)(lds + (unsigned)(w * 20 + a * 5 + c) * 1024u + Q.lv) = acc[c][a];
  lds_bar();
  stamp(7);
#pragma unroll
  for (int i = 0; i < 2; ++i) {
    if (i == 0 || w < 2) {
      const int cc = (w + 8 * i) * 4 + g, tb = cc / 10, c8 = cc - tb * 10;
      const unsigned a = lds + (unsigned)(tb * 5 + (c8 >> 1)) * 1024u + (c8 & 1) * 512u + t * 16u;
      f32x4 lo = ld128f(a), hi = ld128f(a + 256u);
#pragma unroll
      for (int k = 1; k < 8; ++k) { lo += ld128f(a + k * 20480u); hi += ld128f(a + k * 20480u + 256u); }
      const unsigned x0 = xr[i].x, x1 = xr[i].y, x2 = xr[i].z, x3 = xr[i].w;
      const u32x4 o = {pk(lo.x + blo(x0), lo.y + bhi(x0)), pk(lo.z + blo(x1), lo.w + bhi(x1)), pk(hi.x + blo(x2), hi.y + bhi(x2)), pk(hi.z + blo(x3), hi.w + bhi(x3))};
      __builtin_amdgcn_raw_buffer_store_b128(o, ro, ov[i], 0, 0);
    }
  }
}

__global__ __launch_bounds__(512, 2) void fused_block(Params p0) {
  Stamps ST;
  sstamp(29);
  Params p = p0;
  asm volatile("" : "+s"(p.x), "+s"(p.wqkv), "+s"(p.wo), "+s"(p.w1), "+s"(p.w3), "+s"(p.w2), "+s"(p.out), "+s"(p.ws));
  extern __shared__ __attribute__((aligned(16))) char smem[];
  const unsigned lds = (unsigned)(size_t)(LDSP char*)smem;
  const int w = rfl(threadIdx.x >> 6), lane = threadIdx.x & 63;
  const int b = blockIdx.x, xcd = b & 7, i = b >> 3, par = xcd & 1, cgp = xcd >> 1;
  sstamp(30);
  stamp(0);
  u64 tag;
  unsigned warm;
  {
    u64 pc; asm volatile("s_getpc_b64 %0" : "=s"(pc));
    const unsigned o = min((unsigned)(i * 16 + (lane & 15)) * 128u, (unsigned)CODE_KB * 1024u);
    asm volatile("global_load_dword %0, %1, %2" : "=v"(warm) : "v"(o), "s"(pc) : "memory");
  }
  const int mt = xcd * 4 + (i >> 3), n2 = i & 7;
  Job J; job_setup(J, p, b, w, lane);
  P2 Q; p2_setup(Q, p, n2, w, lane);
  int myflag;
  if (i < 18) { const int u = 18 * cgp + i; myflag = (par * 8 + (u & 7)) * 9 + (u >> 3); ffn_tile(p, ST, J, Q, lds, b, w, lane, par, u & 7, u >> 3, n2, myflag, tag); }
  else { const int a = i - 18, hi = a >= 7, jt = a - 7 * hi, hg = 2 * cgp + hi; myflag = 144 + (par * 7 + jt) * 8 + hg; att_tile(p, ST, J, Q, lds, b, w, lane, par, jt, hg, a * 8 + xcd, n2, myflag, tag); }
  land(warm);
  out_tile(p, ST, Q, lds, w, lane, mt, n2, tag);
  if (b == 0 && w == 7) {
#pragma unroll
    for (int k = 0; k < 4; ++k) poll(p.ws + WS_FH + (k * 64 + lane) * 64u, tag);
    if (lane == 0) put_flag(p.ws + WS_E, tag);
  }
}

extern "C" void kernel_launch(void* const* d_in, const int* in_sizes, int n_in, void* d_out, int out_size, void* d_ws, size_t ws_size, hipStream_t stream) {
  Params p;
  p.x = (const char*)d_in[0]; p.wqkv = (const char*)d_in[1]; p.wo = (const char*)d_in[2];
  p.w1 = (const char*)d_in[4]; p.w3 = (const char*)d_in[5]; p.w2 = (const char*)d_in[6];
  p.out = (char*)d_out; p.ws = (char*)d_ws;
  (void)hipFuncSetAttribute((const void*)fused_block, hipFuncAttributeMaxDynamicSharedMemorySize, LDS_BYTES);
  fused_block<<<256, 512, LDS_BYTES, stream>>>(p);
}
```

```cpp
#include <hip/hip_runtime.h>
#include <hip/hip_bf16.h>
#include <utility>

typedef short    s16x4 __attribute__((ext_vector_type(4)));
typedef __bf16   bf16x2 __attribute__((ext_vector_type(2)));
typedef __bf16   bf16x8 __attribute__((ext_vector_type(8)));
typedef float    f32x2 __attribute__((ext_vector_type(2)));
typedef float    f32x4 __attribute__((ext_vector_type(4)));
typedef unsigned u32x2 __attribute__((ext_vector_type(2)));
typedef unsigned u32x4 __attribute__((ext_vector_type(4)));
typedef unsigned long long u64;
typedef __amdgpu_buffer_rsrc_t rsrc_t;
#define LDSP __attribute__((address_space(3)))
#define GAS __attribute__((address_space(1)))
#define DI __device__ __forceinline__
#define GAP() __builtin_amdgcn_sched_barrier(0)
template <int V> using IC = std::integral_constant<int, V>;
template <class F, int... I> DI void sfor_(F&& f, std::integer_sequence<int, I...>) { (f(IC<I>{}), ...); }
template <int N, class F> DI void sfor(F&& f) { sfor_(f, std::make_integer_sequence<int, N>{}); }

#define STAMPS 0
#define ROT 0
#define DPOS 16
#define RPOS 0
#define ADPOS 11
#define ARPOS 0
#define NTOUCH 0
#define LTOUCH 0
#define JEARLY 1
#define NPRE 16
#define PORD 2
#define BAL 0
#define FCHK 14
#define NINL 10
#define NOLD 1
#define KEEPW 8
#define APRE 12
#define PRIO 1
#define ABL 0

constexpr unsigned WS_E  = 0;
constexpr unsigned WS_FH = 4096;
constexpr unsigned WS_FW = 20480;
constexpr unsigned WS_GR = 32768;
constexpr unsigned WS_GC = 65536;
constexpr unsigned CTL_BYTES = WS_GC + 3u * 5376u * 8u;
constexpr unsigned WS_H = 1u << 20;
constexpr unsigned H_BYTES = 32u * 74u * 4096u;
constexpr unsigned WS_W = 16u << 20;
constexpr unsigned W_BYTES = 8u * 74u * 5120u;
constexpr unsigned WS_ST = 32u << 20;
constexpr unsigned OOB = 0x7f000000u;
constexpr int LDS_BYTES = 163840;
constexpr int SC1 = 16;
constexpr int SPIN_CAP = 1 << 18;
constexpr int CODE_KB = 44;

struct Params {
  const char* x; const char* wqkv; const char* wo; const char* w1; const char* w3; const char* w2;
  char* out; char* ws;
};

DI const char* sel(bool c, const char* a, const char* b) { return c ? a : b; }
DI unsigned rfl(unsigned v) { return __builtin_amdgcn_readfirstlane(v); }
DI rsrc_t mk(const void* p, unsigned bytes) { return __builtin_amdgcn_make_buffer_rsrc((void*)p, (short)0, (int)bytes, 0x00020000); }
DI void dma(rsrc_t r, unsigned voff, unsigned soff, unsigned lds) {
  asm volatile("s_mov_b32 m0, %0\n\ts_nop 0\n\tbuffer_load_dwordx4 %1, %2, %3 offen lds" :: "s"(lds), "v"(voff), "s"(r), "s"(soff) : "memory");
}
DI void dma_dev(rsrc_t r, unsigned voff, unsigned soff, unsigned lds) {
  asm volatile("s_mov_b32 m0, %0\n\ts_nop 0\n\tbuffer_load_dwordx4 %1, %2, %3 offen sc1 lds" :: "s"(lds), "v"(voff), "s"(r), "s"(soff) : "memory");
}
template <unsigned SI, unsigned LI> DI void dmai(rsrc_t r, unsigned voff, unsigned soff, unsigned lds) {
  unsigned t;
  asm volatile("s_add_u32 m0, %1, %5\n\ts_add_u32 %0, %4, %6\n\tbuffer_load_dwordx4 %2, %3, %0 offen lds" : "=&s"(t) : "s"(lds), "v"(voff), "s"(r), "s"(soff), "i"(LI), "i"(SI) : "memory", "scc");
}
template <unsigned SI, unsigned LI> DI void dmai_dev(rsrc_t r, unsigned voff, unsigned soff, unsigned lds) {
  unsigned t;
  asm volatile("s_add_u32 m0, %1, %5\n\ts_add_u32 %0, %4, %6\n\tbuffer_load_dwordx4 %2, %3, %0 offen sc1 lds" : "=&s"(t) : "s"(lds), "v"(voff), "s"(r), "s"(soff), "i"(LI), "i"(SI) : "memory", "scc");
}
DI void st128_dev(u32x4 d, rsrc_t r, unsigned voff, unsigned soff) {
  asm volatile("buffer_store_dwordx4 %0, %1, %2, %3 offen sc1\n\ts_nop 1" :: "v"(d), "v"(voff), "s"(r), "s"(soff) : "memory");
}
DI u32x4 hload4(rsrc_t r, unsigned voff, unsigned soff) { u32x4 v; asm volatile("buffer_load_dwordx4 %0, %1, %2, %3 offen" : "=v"(v) : "v"(voff), "s"(r), "s"(soff) : "memory"); return v; }
DI unsigned hload1(rsrc_t r, unsigned voff, unsigned soff) { unsigned v; asm volatile("buffer_load_dword %0, %1, %2, %3 offen" : "=v"(v) : "v"(voff), "s"(r), "s"(soff) : "memory"); return v; }
template <class T> DI void land(T& v) { asm volatile("" : "+v"(v) :: "memory"); }
template <int N> DI void wait_bar() { asm volatile("s_waitcnt vmcnt(%0) lgkmcnt(0)\n\ts_barrier" :: "i"(N) : "memory"); }
template <int N> DI u64 first_bar(const char* e) {
  u64 v; asm volatile("s_load_dwordx2 %0, %1, 0x0\n\ts_waitcnt vmcnt(%2) lgkmcnt(0)\n\ts_barrier" : "=&s"(v) : "s"(e), "i"(N) : "memory"); return v;
}
DI void lds_bar() { asm volatile("s_waitcnt lgkmcnt(0)\n\ts_barrier" ::: "memory"); }
DI unsigned opaque(unsigned v) { asm volatile("" : "+v"(v)); return v; }
DI bf16x8 ld128(unsigned a) { return *(const LDSP bf16x8*)(size_t)a; }
DI u32x4  ld128u(unsigned a) { return *(const LDSP u32x4*)(size_t)a; }
DI f32x4  ld128f(unsigned a) { return *(const LDSP f32x4*)(size_t)a; }
DI u32x2  ld64(unsigned a) { return *(const LDSP u32x2*)(size_t)a; }
DI u32x2  ld64v(unsigned a) { return *(const volatile LDSP u32x2*)(size_t)a; }
DI f32x2  ld64f(unsigned a) { return *(const LDSP f32x2*)(size_t)a; }
DI float  ld32f(unsigned a) { return *(const LDSP float*)(size_t)a; }
DI u32x2  tr64(unsigned a) { s16x4 v = __builtin_amdgcn_ds_read_tr16_b64_v4i16((LDSP s16x4*)(size_t)a); return __builtin_bit_cast(u32x2, v); }
DI bf16x8 cat(u32x2 a, u32x2 b) { u32x4 v = {a.x, a.y, b.x, b.y}; return __builtin_bit_cast(bf16x8, v); }
DI bf16x8 asb(u32x4 v) { return __builtin_bit_cast(bf16x8, v); }
DI f32x4 mfma(bf16x8 a, bf16x8 b, f32x4 c) { return __builtin_amdgcn_mfma_f32_16x16x32_bf16(a, b, c, 0, 0, 0); }
DI void mac(f32x4& c, bf16x8 a, bf16x8 b) { asm volatile("v_mfma_f32_16x16x32_bf16 %0, %1, %2, %0" : "+v"(c) : "v"(a), "v"(b)); }
DI void mac0(f32x4& c, bf16x8 a, bf16x8 b) { asm volatile("v_mfma_f32_16x16x32_bf16 %0, %1, %2, 0" : "=&v"(c) : "v"(a), "v"(b)); }
DI void settle() { asm volatile("s_nop 7\n\ts_nop 1" ::: "memory"); GAP(); }
DI unsigned pk(float lo, float hi) { f32x2 v = {lo, hi}; bf16x2 b = __builtin_convertvector(v, bf16x2); return __builtin_bit_cast(unsigned, b); }
DI float asf(unsigned u) { return __builtin_bit_cast(float, u); }
DI unsigned asu(float f) { return __builtin_bit_cast(unsigned, f); }
DI f32x2 fma2(f32x2 a, f32x2 b, f32x2 c) { return __builtin_elementwise_fma(a, b, c); }
DI f32x2 lo2(f32x4 v) { return __builtin_shufflevector(v, v, 0, 1); }
DI f32x2 hi2(f32x4 v) { return __builtin_shufflevector(v, v, 2, 3); }
DI f32x2 bc2(float v) { return (f32x2){v, v}; }
DI unsigned pk2(f32x2 v) { bf16x2 b = __builtin_convertvector(v, bf16x2); return __builtin_bit_cast(unsigned, b); }
DI float max3(float a, float b, float c) { float r; asm("v_max3_f32 %0, %1, %2, %3" : "=v"(r) : "v"(a), "v"(b), "v"(c)); return r; }
DI float max2(float a, float b) { float r; asm("v_max_f32 %0, %1, %2" : "=v"(r) : "v"(a), "v"(b)); return r; }
DI float blo(unsigned u) { return asf(u << 16); }
DI float bhi(unsigned u) { return asf(u & 0xffff0000u); }
DI float swadd32(float a, float b) { auto r = __builtin_amdgcn_permlane32_swap(asu(a), asu(b), false, false); unsigned r0 = r[0], r1 = r[1]; return asf(r0) + asf(r1); }
DI float swadd16(float a, float b) { auto r = __builtin_amdgcn_permlane16_swap(asu(a), asu(b), false, false); unsigned r0 = r[0], r1 = r[1]; return asf(r0) + asf(r1); }
template <int C> DI float dppadd(float v) { return v + asf(__builtin_amdgcn_update_dpp(0u, asu(v), C, 0xf, 0xf, true)); }
DI float rsum4(float v) { v = swadd16(v, v); return swadd32(v, v); }
DI float wsum(float v) { v = dppadd<0xB1>(v); v = dppadd<0x4E>(v); v = dppadd<0x141>(v); v = dppadd<0x128>(v); return rsum4(v); }
DI float rmax4(float v) {
  auto a = __builtin_amdgcn_permlane16_swap(asu(v), asu(v), false, false); unsigned a0 = a[0], a1 = a[1]; v = max2(asf(a0), asf(a1));
  auto b = __builtin_amdgcn_permlane32_swap(asu(v), asu(v), false, false); unsigned b0 = b[0], b1 = b[1]; return max2(asf(b0), asf(b1));
}
DI u32x4 halffrag(u32x2 a, u32x2 b) {
  auto p = __builtin_amdgcn_permlane32_swap(a.x, b.x, false, false);
  auto q = __builtin_amdgcn_permlane32_swap(a.y, b.y, false, false);
  unsigned p0 = p[0], p1 = p[1], q0 = q[0], q1 = q[1];
  return (u32x4){p0, q0, p1, q1};
}
DI void poll(const char* addr, u64 tag) {
#pragma unroll 1
  for (int i = 0; i < SPIN_CAP; ++i) {
    u32x2 v; asm volatile("global_load_dwordx2 %0, %1, off sc1\n\ts_waitcnt vmcnt(0)" : "=&v"(v) : "v"(addr) : "memory");
    const unsigned lo = v.x, hi = v.y;
    if (__all(lo == (unsigned)tag && hi == (unsigned)(tag >> 32))) break;
    __builtin_amdgcn_s_sleep(1);
  }
}
DI void put_flag(char* addr, u64 tag) { __hip_atomic_store((GAS u64*)addr, tag, __ATOMIC_RELAXED, __HIP_MEMORY_SCOPE_AGENT); }
struct Stamps {};
#define stamp(slot) do {} while (0)
#define cstamp(f) do {} while (0)
#define dump_sv() do {} while (0)
#define sstamp(j) do {} while (0)

struct Job {
  rsrc_t rc; unsigned cv, cstride;
  u32x4 c[4], xa; unsigned xb;
};
DI void job_setup(Job& J, const Params& p, int b, int w, int lane) {
  const int grp = b / 3, th = b - 3 * grp;
  const bool q = grp < 30, one = grp < 57;
  J.cstride = q ? 3840u : 3456u;
  J.rc = mk(sel(q, p.wqkv, sel(one, p.w1, p.w3)), grp < 84 ? 640u * J.cstride : 0u);
  J.cv = __umul24(8 * (3 * w + th) + (lane >> 3), J.cstride) + (unsigned)((q ? grp : one ? grp - 30 : grp - 57) * 128 + (lane & 7) * 16);
}
template <int I> DI void job_loadc(Job& J) { J.c[I] = hload4(J.rc, J.cv, I * 192u * J.cstride); }
DI void job_loadx(Job& J, rsrc_t rx, int b, int w, int lane) {
  const int v = 8 * b + w; const unsigned so = (unsigned)(2 * (v & 1023) + (v >> 10)) * 1280u;
  J.xa = hload4(rx, lane * 16u, so); J.xb = hload1(rx, 1024u + lane * 4u, so);
}
template <class T> DI void pin(T& v) { asm volatile("" : "+v"(v)); }
DI void job_cols(Job& J, unsigned part  , int w, int lane) {
  const int t = lane & 15; const unsigned one = (t & 1) ? 0x3f800000u : 0x00003f80u;
  const bf16x8 sel8 = asb((u32x4){(t >> 1) == 0 ? one : 0u, (t >> 1) == 1 ? one : 0u, (t >> 1) == 2 ? one : 0u, (t >> 1) == 3 ? one : 0u});
  f32x4 d = {0.f, 0.f, 0.f, 0.f};
#pragma unroll
  for (int i = 0; i < 4; ++i) d = mfma(sel8, asb(J.c[i]), d);
  const f32x4 y = {dppadd<0x128>(d.x), dppadd<0x128>(d.y), dppadd<0x128>(d.z), dppadd<0x128>(d.w)};
  *(LDSP f32x4*)(size_t)(part + (lane < 32 ? w * 256u : 2048u) + (lane & 7) * 32u + (lane & 16)) = y;
}
DI float dot2(unsigned a, unsigned b, float c) { return __builtin_amdgcn_fdot2_f32_bf16(__builtin_bit_cast(bf16x2, a), __builtin_bit_cast(bf16x2, b), c, false); }
DI void job_rows(Job& J, rsrc_t rctl, int b, int w, int lane, unsigned tag) {
  const unsigned a0 = J.xa.x, a1 = J.xa.y, a2 = J.xa.z, a3 = J.xa.w, a4 = J.xb, o = 0x3f803f80u;
  const float s = dot2(a4, o, dot2(a3, o, dot2(a2, o, dot2(a1, o, dot2(a0, o, 0.f)))));
  const float q = dot2(a4, a4, dot2(a3, a3, dot2(a2, a2, dot2(a1, a1, dot2(a0, a0, 0.f)))));
  float u = swadd32(s, q);
  u = dppadd<0xB1>(u); u = dppadd<0x4E>(u); u = dppadd<0x141>(u); u = dppadd<0x128>(u); u = swadd16(u, u);
  __builtin_amdgcn_raw_buffer_store_b64((u32x2){asu(u), tag}, rctl, (lane & 31) ? OOB : lane >> 2, WS_GR + (8 * b + w) * 16u, SC1);
}
DI u32x2 regrab(const char* src, unsigned tag) {
  u32x2 v = {0u, 0u};
#pragma unroll 1
  for (int i = 0; i < SPIN_CAP; ++i) {
    asm volatile("global_load_dwordx2 %0, %1, off sc1\n\ts_waitcnt vmcnt(0)" : "=&v"(v) : "v"(src) : "memory");
    const unsigned tg = v.y;
    if (__all(tg == tag)) break;
    __builtin_amdgcn_s_sleep(1);
  }
  return v;
}
DI float colsum(unsigned a, unsigned st, const char* ws, unsigned col, unsigned tag) {
  u32x2 g0 = ld64(a), g1 = ld64(a + st), g2 = ld64(a + 2 * st);
  const unsigned t0 = g0.y, t1 = g1.y, t2 = g2.y;
  if (!__all(t0 == tag && t1 == tag && t2 == tag)) { const char* src = ws + WS_GC + col * 8u; g0 = regrab(src, tag); g1 = regrab(src + 5376 * 8, tag); g2 = regrab(src + 2 * 5376 * 8, tag); }
  const unsigned v0 = g0.x, v1 = g1.x, v2 = g2.x;
  return asf(v0) + asf(v1) + asf(v2);
}
DI f32x2 rowstat(unsigned a, const char* ws, unsigned row, unsigned tag) {
  const u32x4 v = ld128u(a);
  unsigned s = v.x, q = v.z; const unsigned t0 = v.y, t1 = v.w;
  if (!__all(t0 == tag && t1 == tag)) { const char* src = ws + WS_GR + row * 16u; const u32x2 g0 = regrab(src, tag), g1 = regrab(src + 8, tag); s = g0.x; q = g1.x; }
  const float m = asf(s) * (1.f / 640.f);
  return (f32x2){m, __builtin_amdgcn_rsqf(fmaxf(fmaf(asf(q), 1.f / 640.f, -m * m), 0.f) + 1e-5f)};
}
DI void copy_wflags(rsrc_t rctl, unsigned dst, int half, int lane) { dma_dev(rctl, (half == 0 || lane < 10) ? lane * 128u : OOB, WS_FW + half * 8192u, dst + half * 1024u); }
DI void check_wflags(const Params& p, unsigned src, int lane, u64 tag) {
  const u32x2 f = ld64(src + lane * 16u), e = ld64(src + (lane < 10 ? 1024u + lane * 16u : 0u));
  const unsigned f0 = f.x, f1 = f.y, e0 = e.x, e1 = e.y, lo = (unsigned)tag, hi = (unsigned)(tag >> 32);
  if (!__all(f0 == lo && f1 == hi && e0 == lo && e1 == hi)) { poll(p.ws + WS_FW + lane * 128u, tag); poll(p.ws + WS_FW + (lane < 10 ? 64 + lane : 0) * 128u, tag); }
}
DI void job_pubc(unsigned part, rsrc_t rctl, int b, int lane, unsigned tag) {
  float s = 0.f;
#pragma unroll
  for (int k = 0; k < 8; ++k) s += ld32f(part + k * 256u + lane * 4u);
  const int grp = b / 3, th = b - 3 * grp;
  __builtin_amdgcn_raw_buffer_store_b64((u32x2){asu(s), tag}, rctl, grp < 84 ? lane * 8u : OOB, WS_GC + (unsigned)(th * 5376 + grp * 64) * 8u, SC1);
}

struct P2 { rsrc_t rq; unsigned st[10], wo[10], lv; };
DI void p2_setup(P2& Q, int n, int w) {
  const int hs = w < 2 ? 20 + 7 * w : w < 4 ? 22 + 6 * w : 18 + 7 * w;
#pragma unroll
  for (int s = 0; s < 6; ++s) Q.st[s] = hs + s;
  Q.st[6] = (w & 6) == 2 ? 16 + w : hs + 6; Q.st[7] = 2 * w; Q.st[8] = 2 * w + 1;
  Q.st[9] = w < 2 ? 16 + w : 0x10000;
#pragma unroll
  for (int s = 0; s < 10; ++s) Q.wo[s] = (n * 74u + Q.st[s]) * 5120u;
}

enum { EV_NONE = 0, EV_W = 1000  , EV_X = 300  , EV_P = 500  ,
       EV_JC = 10  , EV_JX = 14, EV_FCOPY = 16, EV_GCOPY = 17, EV_TDMA = 18, EV_TST = 19, EV_R = 600   };
struct Sched { int n; int j[300], m[300], c[300]; };
constexpr int J_ROWS = PORD >= 6 ? 3 : PORD ? 2 : JEARLY ? 3 : 6, J_COLS = PORD >= 6 ? 5 : PORD ? 4 : JEARLY ? 8 : 9, J_PUB = J_COLS + 2, J_GRAN = 17, F_CHK = FCHK, A_TR = 4, A_FLAG = 8, A_ROWS = PORD >= 6 ? 8 : PORD ? 2 : 6, A_COLS = PORD ? 6 : J_COLS  , A_CHK = 14;
constexpr Sched make_sched(bool ffn) {
  Sched S{}; int n = 0;
  const int NW = ffn ? 3 : 2;
  auto put = [&](int j, int m, int c) { S.j[n] = j; S.m[n] = m; S.c[n] = c; ++n; };
  for (int j = -1; j < 20; ++j) {
    int L[24] = {}; int k = 0;
    auto W = [&](int s) { for (int i = 0; i < NW; ++i) { if (s < 20) L[k++] = EV_W + 4 * s + i; else if (ffn) L[k++] = EV_P + (s & 3) * 3 + i; } };
    auto X = [&](int s, int h) { if (s < 10) L[k++] = EV_X + 2 * s + h; else if (ffn) L[k++] = EV_P + 12 + (s % 3) * 2 + h; };
    auto E = [&](int c) { L[k++] = c; };
    if (j == -1) {
      if (PORD == 0) { W(0); X(0, 0); X(0, 1); W(1); W(2); X(1, 0); X(1, 1); }
      if (PORD == 1) { E(EV_JC); W(0); X(0, 0); X(0, 1); E(EV_JX); W(1); E(EV_JC + 1); W(2); X(1, 0); X(1, 1); E(EV_JC + 2); E(EV_JC + 3); }
      if (PORD == 2) { W(0); X(0, 0); X(0, 1); E(EV_JC); E(EV_JX); W(1); E(EV_JC + 1); W(2); X(1, 0); X(1, 1); E(EV_JC + 2); E(EV_JC + 3); }
      if (PORD == 3) { E(EV_JC); E(EV_JX); E(EV_JC + 1); E(EV_JC + 2); E(EV_JC + 3); W(0); X(0, 0); X(0, 1); W(1); W(2); X(1, 0); X(1, 1); }
      if (PORD == 4) { E(EV_JC); W(0); X(0, 0); X(0, 1); W(1); E(EV_JX); E(EV_JC + 1); E(EV_JC + 2); E(EV_JC + 3); W(2); X(1, 0); X(1, 1); }
      if (PORD == 5) { W(0); X(0, 0); X(0, 1); W(1); W(2); X(1, 0); X(1, 1); E(EV_JC); E(EV_JX); E(EV_JC + 1); E(EV_JC + 2); E(EV_JC + 3); }
      if (PORD == 6) { W(0); X(0, 0); X(0, 1); E(EV_JC); W(1); }
      if (PORD == 7) { W(0); X(0, 0); X(0, 1); W(1); E(EV_JC); }
      if (PORD == 8) { W(0); X(0, 0); X(0, 1); E(EV_JC); E(EV_JX); W(1); }
    }
    else if (PORD >= 6 && j == 0) { W(2); X(1, 0); X(1, 1); if (PORD != 8) E(EV_JX); E(EV_JC + 1); W(3); }
    else if (PORD >= 6 && j == 1) { X(2, 0); X(2, 1); W(4); E(EV_JC + 2); W(5); X(3, 0); }
    else if (PORD >= 6 && j == 2) { E(EV_JC + 3); W(6); X(3, 1); }
    else if (j == 0) { W(3); X(2, 0); X(2, 1); W(4); }
    else { W(j + 4); if (j % 2 == 0) X(j / 2 + 2, 1); else X((j - 1) / 2 + 3, 0); }
    if (ffn && j == 19) L[k++] = EV_P + 13;
    const int k0 = k;
    if (ffn && j > F_CHK) for (int i = (j - F_CHK - 1) * NINL / (19 - F_CHK); i < (j - F_CHK) * NINL / (19 - F_CHK); ++i) put(j, 3 + 6 * (i & 1), EV_R + 20 + i);
    if (!ffn) {
      if (j == 15) { L[k++] = EV_P + 15; }
      if (j == 16) { L[k++] = EV_P + 16; L[k++] = EV_P + 17; L[k++] = EV_P + 10; L[k++] = EV_P + 11; }
      if (j == 17) { L[k++] = EV_P + 12; L[k++] = EV_P + 13; L[k++] = EV_P + 14; L[k++] = EV_R + 20; }
      if (j == 18) for (int i = 21; i < 25; ++i) L[k++] = EV_R + i;
      if (j == 19) for (int i = 6; i < 10; ++i) L[k++] = EV_P + i;
    }
    const int last = ffn ? 23 : 14;
    for (int i = 0; i < k; ++i) put(j, j == -1 ? 0 : (PORD >= 6 && j < 2) ? (ffn ? 1 + 2 * i : 1 + i + i / 2) : j == 0 ? 2 + 2 * i : ffn ? (k > 4 && DPOS > 14 ? 14 : DPOS) + 2 * i : (j >= 15 ? 2 + 3 * i : ADPOS + i), L[i]);
    if (PORD) {} else if (JEARLY) {
      if (j == -1) { put(j, 0, EV_JC); put(j, 0, EV_JX); }
      if (j == 1) put(j, last, EV_JC + 1);
      if (j == 3) put(j, last, EV_JC + 2);
      if (j == 5) put(j, last, EV_JC + 3);
    } else {
      if (j == 1) put(j, last, EV_JC);
      if (j == 3) { put(j, last, EV_JX); put(j, last, EV_JC + 1); }
      if (j == 5) put(j, last, EV_JC + 2);
      if (j == 6) put(j, last, EV_JC + 3);
    }
    if (j == (ffn ? F_CHK : A_CHK) - 3) put(j, last, EV_FCOPY);
    if (j == J_GRAN - 3) put(j, last, EV_GCOPY);
    if (!ffn && j == (PORD >= 6 ? 1 : 0)) put(j, last, EV_TDMA);
    if (!ffn && j == A_TR + 1) put(j, 15, EV_TST);
  }
  for (int a = 1; a < n; ++a)
    for (int c = a; c > 0 && S.j[c - 1] == S.j[c] && S.m[c - 1] > S.m[c]; --c) {
      const int tm = S.m[c], tc = S.c[c]; S.m[c] = S.m[c - 1]; S.c[c] = S.c[c - 1]; S.m[c - 1] = tm; S.c[c - 1] = tc; }
  S.n = n; return S;
}
constexpr int ev_ops(int c, bool v7) {
  if (c >= EV_W) return (v7 && (c - EV_W) % 4 == 1) ? 0 : 1;
  if (c >= EV_P) return 1;
  if (c >= EV_X) return v7 ? 2 : 1;
  if (c >= EV_JC && c < EV_JX) return 1;
  return c == EV_JX ? 2 : 0;
}
constexpr int ev_due(int c, bool ffn) {
  if (c >= EV_W) return (c - EV_W) / 4 - 1;
  if (c >= EV_P) return 99;
  if (c >= EV_X) return 2 * ((c - EV_X) / 2) - 1;
  if (c >= EV_JC && c < EV_JX) return J_COLS;
  return c == EV_JX ? J_ROWS : c == EV_FCOPY ? (ffn ? F_CHK : A_CHK) : c == EV_GCOPY ? J_GRAN : c == EV_TDMA ? A_TR : c == EV_TST ? A_FLAG : 99;
}
constexpr int wait_n(const Sched& S, bool ffn, bool v7, int j) {
  int last = -1, end = 0;
  for (int i = 0; i < S.n; ++i) if (S.j[i] < j || (j == -1 && S.j[i] == -1)) { end = i + 1; if (ev_due(S.c[i], ffn) == j) last = i; }
  if (last < 0) return 63;
  int n = 0; for (int i = last + 1; i < end; ++i) n += ev_ops(S.c[i], v7);
  return n > 63 ? 63 : n;
}
constexpr Sched F_S = make_sched(true), A_S = make_sched(false);
constexpr int waitF(int j) { return wait_n(F_S, true, false, j); }
constexpr int waitA(int j) { return wait_n(A_S, false, false, j) < wait_n(A_S, false, true, j) ? wait_n(A_S, false, false, j) : wait_n(A_S, false, true, j); }
constexpr int ev_at(const Sched& S, int j, int m, int k) {
  for (int i = 0; i < S.n; ++i) if (S.j[i] == j && S.m[i] == m) { if (k == 0) return S.c[i]; --k; }
  return EV_NONE;
}

template <unsigned IMM> DI void ldev(u32x4& d, rsrc_t r, unsigned voff, unsigned soff) {
  asm volatile("buffer_load_dwordx4 %0, %1, %2, %3 offen offset:%4 sc1" : "=v"(d) : "v"(voff), "s"(r), "s"(soff), "i"(IMM) : "memory");
}
template <int F> DI void loadW(u32x4 (&Wf)[50], const P2& Q) {
  if constexpr (F % 5 < 4) ldev<(F % 5) * 1024u>(Wf[F], Q.rq, Q.lv, Q.wo[F / 5]); else ldev<3072u>(Wf[F], Q.rq, Q.lv, Q.wo[F / 5] + 1024u);
}
template <int S, int NF, int KEEP> DI void loadW_if(unsigned go, u32x4 (&Wf)[50], const P2& Q) {
  unsigned t;
  if constexpr (NF == 5)
    asm volatile("s_cmp_eq_u32 %9, 0\n\ts_cbranch_scc1 1f\n\ts_add_u32 %5, %8, 0x400\n\t"
                 "buffer_load_dwordx4 %0, %6, %7, %8 offen sc1\n\tbuffer_load_dwordx4 %1, %6, %7, %8 offen offset:1024 sc1\n\tbuffer_load_dwordx4 %2, %6, %7, %8 offen offset:2048 sc1\n\t"
                 "buffer_load_dwordx4 %3, %6, %7, %8 offen offset:3072 sc1\n\tbuffer_load_dwordx4 %4, %6, %7, %5 offen offset:3072 sc1\n\ts_waitcnt vmcnt(%10)\n1:"
                 : "+v"(Wf[5 * S]), "+v"(Wf[5 * S + 1]), "+v"(Wf[5 * S + 2]), "+v"(Wf[5 * S + 3]), "+v"(Wf[5 * S + 4]), "=&s"(t)
                 : "v"(Q.lv), "s"(Q.rq), "s"(Q.wo[S]), "s"(go), "i"(KEEP) : "memory", "scc");
  else
    asm volatile("s_cmp_eq_u32 %6, 0\n\ts_cbranch_scc1 1f\n\ts_add_u32 %2, %5, 0x400\n\t"
                 "buffer_load_dwordx4 %0, %3, %4, %5 offen offset:3072 sc1\n\tbuffer_load_dwordx4 %1, %3, %4, %2 offen offset:3072 sc1\n\ts_waitcnt vmcnt(%7)\n1:"
                 : "+v"(Wf[5 * S + 3]), "+v"(Wf[5 * S + 4]), "=&s"(t) : "v"(Q.lv), "s"(Q.rq), "s"(Q.wo[S]), "s"(go), "i"(KEEP) : "memory", "scc");
}
template <class T> DI void fresh(T& v) { asm volatile("" : "=v"(v)); }
template <int N> DI void waitv() { asm volatile("s_waitcnt vmcnt(%0)" :: "i"(N) : "memory"); }
template <int S0, int S1> DI void out_tile(const Params& p, Stamps& ST, const P2& Q, u32x4 (&Wf)[50], unsigned late, unsigned lds, int w, int lane, int mt, int n, u64 tag) {
  const rsrc_t rh = mk(p.ws + WS_H, H_BYTES);
  const int t = lane & 15, g = lane >> 4;
  const rsrc_t rx = mk(p.x, 2048u * 1280u), ro = mk(p.out, 2048u * 1280u);
  stamp(3); sstamp(34);
  const int b = blockIdx.x;
  const bool last = blockIdx.x == 0 && w >= 4;
  unsigned ov[2]; u32x4 xr[2];
  unsigned is3; asm("s_cmp_eq_u32 %1, 3\n\ts_cselect_b32 %0, 1, 0" : "=s"(is3) : "s"(w) : "scc");
  sfor<S1 - S0>([&](auto K) { loadW_if<S0 + K.value, 5, 63>(late, Wf, Q); });
  sfor<9 - S1>([&](auto K) { loadW_if<S1 + K.value, 5, 63>(w ^ 3, Wf, Q); });
  if (w == 3) poll(p.ws + WS_FH + ((mt >> 1) * 9 + (lane < 9 ? lane : 0)) * 64u, tag);
  sfor<9 - S1>([&](auto K) { loadW_if<S1 + K.value, 5, 63>(is3, Wf, Q); });
  const int jl = (mt & 15) ? (4 * (mt & 15) - 1) / 9 : 0, jh = (4 * (mt & 15) + 2) / 9;
  const unsigned fa = WS_FH + (144 + ((mt >> 4) * 7 + jl) * 8) * 64u, fn = (jh - jl + 1) * 8;
  u32x4 Hf[3][4];
  constexpr auto slot = [](int q) { return q < 7 ? q : q == 7 ? 9 : q - 1; };
  constexpr auto bi = [](int q) { return q < 6 ? (q & 1) : (q - 4) % 3; };
  auto loadH = [&](auto Qp, auto I) { ldev<I.value * 1024u>(Hf[bi(Qp.value)][I.value], rh, Q.lv, (mt * 74u + Q.st[slot(Qp.value)]) * 4096u); };
  const unsigned lq = opaque(lds + w * 1024u + Q.lv), lq2 = opaque(lq + 8 * 8192u), lq3 = opaque(lq + 16 * 8192u);
  auto row = [&](int r) { return (r < 8 ? lq : r < 16 ? lq2 : lq3) + (r & 7) * 8192u; };
  lds_bar();
  stamp(4); stamp(5); sstamp(35);
  if (BAL >= 2 && w >= 4) __builtin_amdgcn_s_setprio(1);
  sfor<4>([&](auto I) { loadH(IC<0>{}, I); }); sfor<4>([&](auto I) { loadH(IC<1>{}, I); });
  f32x4 acc[5][4];
  constexpr auto nX = [](int q) { return q == 1 ? 1 : q == 4 ? 5 : q == 5 ? 2 : 0; };
  sfor<8>([&](auto Qp) {
    constexpr int q = Qp.value, s = slot(q);
    waitv<(q >= 2 ? nX(q - 2) : 0) + 4 + (q >= 1 ? nX(q - 1) : 0)>();
    sstamp(36 + q);
    land(Hf[bi(q)][0]); land(Hf[bi(q)][1]); land(Hf[bi(q)][2]); land(Hf[bi(q)][3]);
    if constexpr (q == 4) {
      const u32x2 f = ld64(lq); const unsigned f0 = f.x, f1 = f.y, l4 = opaque(Q.lv) >> 4;
      if (!__all(l4 >= fn || (f0 == (unsigned)tag && f1 == (unsigned)(tag >> 32)))) poll(p.ws + fa + (l4 < fn ? l4 : 0) * 64u, tag);
    }
    if constexpr (s == 9) {
      sfor<4>([&](auto I) { loadH(IC<q + 2>{}, I); });
      sfor<5>([&](auto C) { land(Wf[45 + C.value]); });
      if (w < 2) sfor<5>([&](auto C) {
#pragma unroll
        for (int a = 0; a < 4; ++a) mac(acc[C.value][a], asb(Wf[45 + C.value]), asb(Hf[bi(q)][a]));
      });
    } else {
      bf16x8 wf[5];
      auto get = [&](auto C) { constexpr int f = s * 5 + C.value;
        if constexpr (f >= 20) { land(Wf[f]); wf[C.value] = asb(Wf[f]); } else wf[C.value] = ld128(row(f)); };
      get(IC<0>{}); get(IC<1>{});
      sfor<5>([&](auto C) {
        if constexpr (C.value < 3) get(IC<C.value + 2>{});
#pragma unroll
        for (int a = 0; a < 4; ++a) { if constexpr (q == 0) mac0(acc[C.value][a], wf[C.value], asb(Hf[0][a])); else mac(acc[C.value][a], wf[C.value], asb(Hf[bi(q)][a])); }
        if constexpr (q >= 4 && C.value < 4) loadH(IC<q + 2>{}, C);
      });
      if constexpr (q < 4) sfor<4>([&](auto I) { loadH(IC<q + 2>{}, I); });
    }
    if constexpr (BAL >= 2 && q == BAL) __builtin_amdgcn_s_setprio(0);
    if constexpr (BAL == 1 && (q == 1 || q == 3 || q == 5)) __builtin_amdgcn_s_barrier();
    if constexpr (q == 1) dma_dev(mk(p.ws, CTL_BYTES), (opaque(Q.lv) >> 4) < fn ? Q.lv * 4u : OOB, fa, lds + w * 1024u);
    if constexpr (q == 4) sfor<5>([&](auto K) { loadW<45 + K.value>(Wf, Q); });
    if constexpr (q == 5) {
      const unsigned l2 = opaque(Q.lv) >> 4;
#pragma unroll
      for (int i = 0; i < 2; ++i) {
        const int cc = (w + 8 * i) * 4 + (l2 >> 4), tb = (cc * 26) >> 8, c8 = cc - tb * 10;
        const int B = 4 * mt + tb, tok = 2 * ((B & 63) * 16 + (l2 & 15)) + (B >> 6);
        ov[i] = (i == 0 || w < 2) ? (unsigned)(tok * 1280 + n * 160 + c8 * 16) : OOB;
      }
      xr[0] = hload4(rx, ov[0], 0u); xr[1] = hload4(rx, ov[1], 0u);
    }
    if constexpr (q == 5) { if (last) dma_dev(mk(p.ws, CTL_BYTES), lane * 64u, WS_FH + (w - 4) * 4096u, lds + w * 1024u); }
    GAP();
  });
  waitv<3>();
  sstamp(44); sstamp(45);
  land(xr[0]); land(xr[1]);
  if (last) {
    const u32x2 f = ld64(lq); const unsigned f0 = f.x, f1 = f.y;
    if (!__all(f0 == (unsigned)tag && f1 == (unsigned)(tag >> 32))) poll(p.ws + WS_FH + ((w - 4) * 64 + lane) * 64u, tag);
  }
  sfor<10>([&](auto K) { land(Wf[35 + K.value]); });
  sfor<4>([&](auto A) {
    constexpr int a = A.value;
    if constexpr (a > 0) waitv<3 - a>();
    land(Hf[1][a]); land(Hf[2][a]);
    sfor<5>([&](auto C) { mac(acc[C.value][a], asb(Wf[35 + C.value]), asb(Hf[1][a])); GAP(); });
    sfor<5>([&](auto C) { mac(acc[C.value][a], asb(Wf[40 + C.value]), asb(Hf[2][a]));
      if constexpr (a > 0) *(LDSP f32x4*)(size_t)row(5 * (a - 1) + C.value) = acc[C.value][a - 1];
      GAP(); });
  });
  settle();
  stamp(6); sstamp(46);
#pragma unroll
  for (int c = 0; c < 5; ++c) *(LDSP f32x4*)(size_t)row(15 + c) = acc[c][3];
  lds_bar();
  if (last && w == 7 && lane == 0) put_flag(p.ws + WS_E, tag);
  stamp(7); sstamp(47);
#pragma unroll
  for (int i = 0; i < 2; ++i) {
    if (i == 0 || w < 2) {
      const int cc = (w + 8 * i) * 4 + g, tb = cc / 10, c8 = cc - tb * 10;
      const unsigned a = lds + (unsigned)(tb * 5 + (c8 >> 1)) * 8192u + (c8 & 1) * 512u + t * 16u;
      f32x4 lo[8], hi[8];
#pragma unroll
      for (int k = 0; k < 8; ++k) { lo[k] = ld128f(a + k * 1024u); hi[k] = ld128f(a + k * 1024u + 256u); }
      const f32x4 l = ((lo[0] + lo[1]) + (lo[2] + lo[3])) + ((lo[4] + lo[5]) + (lo[6] + lo[7])), hh = ((hi[0] + hi[1]) + (hi[2] + hi[3])) + ((hi[4] + hi[5]) + (hi[6] + hi[7]));
      const unsigned x0 = xr[i].x, x1 = xr[i].y, x2 = xr[i].z, x3 = xr[i].w;
      const u32x4 o = {pk(l.x + blo(x0), l.y + bhi(x0)), pk(l.z + blo(x1), l.w + bhi(x1)), pk(hh.x + blo(x2), hh.y + bhi(x2)), pk(hh.z + blo(x3), hh.w + bhi(x3))};
      __builtin_amdgcn_raw_buffer_store_b128(o, ro, ov[i], 0, 0);
    }
  }
  sstamp(48);
  dump_sv();
}

constexpr unsigned F_WS = 24576, F_XR = 98304, F_XS = 16384, F_EX = 147456;
constexpr unsigned FX_G = F_EX, FX_CS = F_EX + 11264, FX_ST = FX_CS + 1536, FX_FW = FX_ST + 1024, FX_JP = F_EX;
DI void ffn_tile(const Params& p, Stamps& ST, unsigned lds, int b, int w, int lane, int par, int rtl, int ct, int mt, int n2, int myflag, u64& tag) {
  P2 Q;
  const int mg = w >> 2, ng = w & 3;
  const int t = lane & 15, g = lane >> 4;
  const rsrc_t rx = mk(p.x, 2048u * 1280u), r1 = mk(p.w1, 640u * 3456u), r3 = mk(p.w3, 640u * 3456u);
  const rsrc_t rm = mk(sel(mg, p.w3, p.w1), 640u * 3456u), rctl = mk(p.ws, CTL_BYTES);
  const unsigned wv = (unsigned)(8 * ng + (lane >> 3)) * 3456u + (unsigned)((((lane & 7) >> 1) ^ (((lane >> 4) & 1) | ((ng & 1) << 1))) * 32 + (lane & 1) * 16);
  const unsigned ws0 = ct * 384u + mg * 128u, ws1 = ct * 384u + (mg ? 0u : 256u);
  const unsigned xv = (unsigned)(2 * (8 * w + (lane >> 3))) * 1280u + (unsigned)(((lane & 7) ^ (((lane >> 4) & 3) | ((w & 1) << 2))) * 16);
  const unsigned xs0 = (unsigned)(2 * 128 * rtl + par) * 1280u;
  const unsigned lw = lds + w * 1024u;
  Job J; u32x4 Wf[50];
  auto event = [&](auto C) {
    constexpr int c = C.value;
    if constexpr (c >= EV_W) {
      constexpr int s = (c - EV_W) / 4, i = (c - EV_W) % 4;
      dmai<s * 32u * 3456u + (i == 2 ? 128u : 0u), ((s & 3) * 3 + i) * 8192u>(i == 0 ? r1 : i == 1 ? rm : r3, wv, i == 1 ? ws1 : ws0, lw);
    } else if constexpr (c >= EV_R) loadW<c - EV_R>(Wf, Q);
    else if constexpr (c >= EV_P) dmai_dev<((c - EV_P) % 5) * 1024u, (c - EV_P) * 8192u>(Q.rq, Q.lv, Q.wo[(c - EV_P) / 5], lw);
    else if constexpr (c >= EV_X) {
      constexpr int s = (c - EV_X) / 2, h = (c - EV_X) % 2;
      dmai<s * 128u + h * (128u * 1280u), (12 + (s % 3) * 2 + h) * 8192u>(rx, xv, xs0, lw);
    } else if constexpr (c >= EV_JC && c < EV_JX) { if constexpr (c == EV_JC) job_setup(J, p, b, w, lane); job_loadc<c - EV_JC>(J); }
    else if constexpr (c == EV_JX) job_loadx(J, rx, b, w, lane);
    else if constexpr (c == EV_FCOPY) { if (w < 2) copy_wflags(rctl, lds + FX_FW, w, lane); }
    else if constexpr (c == EV_GCOPY) {
      if (w < 6) {
        const unsigned so = WS_GC + (unsigned)((w % 3) * 5376 + 1920 + (w / 3) * 1728 + 192 * ct) * 8u;
        dma_dev(rctl, Q.lv, so, lds + FX_G + w * 1536u);
        if (lane < 32) dma_dev(rctl, Q.lv, so + 1024u, lds + FX_G + w * 1536u + 1024u);
      } else dma_dev(rctl, Q.lv, WS_GR + (unsigned)(par * 1024 + rtl * 128 + (w - 6) * 64) * 16u, lds + FX_G + 9216u + (w - 6) * 1024u);
    }
  };
  sstamp(31);
  sfor<18>([&](auto K) { event(IC<ev_at(F_S, -1, 0, K.value)>{}); GAP(); });
  sstamp(32);
  Q.rq = mk(p.ws + WS_W, W_BYTES); Q.lv = lane * 16u;
  unsigned xa[2], wa[2][3];
  xa[0] = lds + F_XR + (unsigned)(64 * mg + t) * 128u + (unsigned)((g ^ (t >> 1)) * 16); xa[1] = opaque(xa[0] ^ 64u);
  {
    const int q = t >> 2, pp = t & 3, f = (q >> 1) | ((g & 1) << 1);
#pragma unroll
    for (int i = 0; i < 3; ++i) { const int fb = 3 * ng + i; wa[0][i] = lds + (fb >> 2) * 4096u + (8 * g + q) * 128u + (((fb & 3) ^ f) * 32) + 8 * pp; wa[1][i] = opaque(wa[0][i] + 2 * F_WS); }
  }
  f32x4 acc[6][4];
  bf16x8 X[2][4]; u32x2 W[2][12];
  auto rd = [&](auto S, auto R) {
    constexpr int s = S.value, r = R.value;
    if constexpr (r < 4) X[s & 1][r] = ld128(xa[s & 1] + ((s >> 1) % 3) * F_XS + r * 2048u);
    else { constexpr int k = r - 4, bb = k >> 1; W[s & 1][k] = tr64(wa[(s >> 1) & 1][bb % 3] + (s & 1) * F_WS + (bb / 3) * 12288u + (k & 1) * 512u); }
  };
  tag = first_bar<waitF(-1)>(p.ws) + 1;
  stamp(1); cstamp(c0); sstamp(33);
  sfor<16>([&](auto R) { rd(IC<0>{}, R); });
  sfor<20>([&](auto JC) {
    constexpr int j = JC.value;
    wait_bar<waitF(j)>();
    sstamp(j);
    constexpr int lead = (j == J_ROWS + 1 || j == J_COLS + 1 || j == J_GRAN + 1 || j == F_CHK) ? 1 : 0;
    if constexpr (PRIO && lead) { if (mg) __builtin_amdgcn_s_setprio(1); }
    if constexpr (j == J_ROWS) { land(J.xa); land(J.xb); }
    if constexpr (j == J_COLS) { land(J.c[0]); land(J.c[1]); land(J.c[2]); land(J.c[3]); }
    if constexpr (j == F_CHK) { int w2 = w; asm volatile("" : "+s"(w2)); p2_setup(Q, n2, w2); }
    GAP();
    sfor<24>([&](auto MC) {
      constexpr int m = MC.value, bb = m / 4, a = m % 4;
      if constexpr (j == 0) mac0(acc[bb][a], cat(W[0][2 * bb], W[0][2 * bb + 1]), X[0][a]); else mac(acc[bb][a], cat(W[j & 1][2 * bb], W[j & 1][2 * bb + 1]), X[j & 1][a]);
      if constexpr (j < 19 && m >= RPOS && m < RPOS + 16) rd(IC<j + 1>{}, IC<m - RPOS>{});
      sfor<3>([&](auto K) { event(IC<ev_at(F_S, j, m, K.value)>{}); });
      GAP();
    });
    if constexpr (j == J_ROWS || j == J_ROWS + 1) { if (mg == j - J_ROWS) job_rows(J, rctl, b, w, lane, (unsigned)tag); }
    if constexpr (j == J_COLS || j == J_COLS + 1) { if (mg == j - J_COLS) job_cols(J, lds + FX_JP, w, lane); }
    if constexpr (j == J_PUB) { if (w == 1) job_pubc(lds + FX_JP, rctl, b, lane, (unsigned)tag); }
    if constexpr (j == F_CHK) { if (w == 7) check_wflags(p, lds + FX_FW, lane, tag); }
    if constexpr (j == J_GRAN || j == J_GRAN + 1) {
      if (mg == j - J_GRAN) {
        const int tid = w * 64 + lane;
        if (w < 6) {
          const int up = w >= 3, c = tid - 192 * up;
          *(LDSP float*)(size_t)(lds + FX_CS + tid * 4u) = colsum(lds + FX_G + up * 4608u + c * 8u, 1536u, p.ws, 1920 + up * 1728 + 192 * ct + c, (unsigned)tag);
        } else *(LDSP f32x2*)(size_t)(lds + FX_ST + (tid - 384) * 8u) = rowstat(lds + FX_G + 9216u + (tid - 384) * 16u, p.ws, par * 1024 + rtl * 128 + tid - 384, (unsigned)tag);
      }
    }
    if constexpr (PRIO && lead) __builtin_amdgcn_s_setprio(0);
    GAP();
  });
  settle();
  stamp(2); cstamp(c1); sstamp(20);
  f32x4 cG[3], cU[3]; f32x2 stt[4];
#pragma unroll
  for (int i = 0; i < 3; ++i) { cG[i] = ld128f(lds + FX_CS + ((3 * ng + i) * 16 + 4 * g) * 4u); cU[i] = ld128f(lds + FX_CS + 768u + ((3 * ng + i) * 16 + 4 * g) * 4u); }
#pragma unroll
  for (int a = 0; a < 4; ++a) stt[a] = ld64f(lds + FX_ST + (64 * mg + 16 * a + t) * 8u);
  const rsrc_t rh = mk(p.ws + WS_H, H_BYTES);
  const unsigned hv = (unsigned)(lane >> 5) * 1024u + (lane & 31) * 16u;
  const unsigned hs = (unsigned)(2 * (8 * par + rtl) + mg) * (74u * 4096u);
  u32x4 HO[2][3];
  sfor<2>([&](auto AP) {
    constexpr int ap = AP.value;
    u32x2 P[2][3];
    sfor<2>([&](auto A2) {
      constexpr int a2 = A2.value, a = 2 * ap + a2;
      const f32x2 st = stt[a];
      const f32x2 nm = bc2(-st.x), k1 = bc2(st.y * -1.44269504089f), ik = bc2(__builtin_amdgcn_rcpf(st.y * st.y));
      f32x2 G[6], U[6], E[6];
#pragma unroll
      for (int i = 0; i < 3; ++i) { G[2 * i] = fma2(nm, lo2(cG[i]), lo2(acc[i][a])); G[2 * i + 1] = fma2(nm, hi2(cG[i]), hi2(acc[i][a])); }  GAP();
#pragma unroll
      for (int k = 0; k < 6; ++k) E[k] = G[k] * k1;
#pragma unroll
      for (int i = 0; i < 3; ++i) { U[2 * i] = fma2(nm, lo2(cU[i]), lo2(acc[3 + i][a])); U[2 * i + 1] = fma2(nm, hi2(cU[i]), hi2(acc[3 + i][a])); }
      GAP();
#pragma unroll
      for (int k = 0; k < 6; ++k) E[k] = (f32x2){__builtin_amdgcn_exp2f(E[k].x), __builtin_amdgcn_exp2f(E[k].y)};  GAP();
#pragma unroll
      for (int k = 0; k < 6; ++k) { E[k] = fma2(E[k], ik, ik); G[k] = G[k] * U[k]; }  GAP();
#pragma unroll
      for (int k = 0; k < 6; ++k) E[k] = (f32x2){__builtin_amdgcn_rcpf(E[k].x), __builtin_amdgcn_rcpf(E[k].y)};  GAP();
#pragma unroll
      for (int k = 0; k < 6; ++k) G[k] = G[k] * E[k];  GAP();
#pragma unroll
      for (int i = 0; i < 3; ++i) P[a2][i] = (u32x2){pk2(G[2 * i]), pk2(G[2 * i + 1])};
      GAP();
    });
#pragma unroll
    for (int i = 0; i < 3; ++i) { HO[ap][i] = halffrag(P[0][i], P[1][i]); pin(HO[ap][i]); }
  });
#pragma unroll
  for (int k = 0; k < 6; ++k) {
    const int fb = 12 * ct + 3 * ng + k % 3;
    st128_dev(HO[k / 3][k % 3], rh, hv, hs + (20 + (fb >> 1)) * 4096u + (k / 3) * 2048u + (fb & 1) * 512u);
  }
  sfor<25 - NINL>([&](auto K) { fresh(Wf[20 + NINL + K.value]); });
  sfor<NOLD>([&](auto K) { loadW_if<4 + NINL / 5 + K.value, 5, KEEPW>(1 - mg, Wf, Q); });
  sstamp(21);
  if (mg) wait_bar<0>(); else wait_bar<5 * NOLD>();
  sstamp(22); sstamp(23); sstamp(24); sstamp(25); sstamp(26); sstamp(27); sstamp(28);
  if (w == 0 && lane == 0) put_flag(p.ws + WS_FH + myflag * 64u, tag);
  dmai_dev<3072u, 18 * 8192u>(Q.rq, Q.lv, Q.wo[3], lw); dmai_dev<4096u, 19 * 8192u>(Q.rq, Q.lv, Q.wo[3], lw);
  out_tile<4 + NINL / 5, 4 + NINL / 5 + NOLD>(p, ST, Q, Wf, mg, lds, w, lane, mt, n2, tag);
}

constexpr unsigned A_WS = 15360, A_XR = 61440, A_XS = 20480, A_JOB = 122880;
constexpr unsigned AX_G = 147456, AX_JP = AX_G, AX_FW = AX_G + 9216, AX_CS = AX_FW + 2048, AX_ST = AX_CS + 1024, AX_Z = AX_ST + 1536;
DI void att_tile(const Params& p, Stamps& ST, unsigned lds, int b, int w, int lane, int par, int jt, int hg, int job, int mt, int n2, int myflag, u64& tag) {
  P2 Q;
  const int mg = w >> 2, h = (w + mg) & 3;
  const int t = lane & 15, g = lane >> 4;
  const rsrc_t rx = mk(p.x, 2048u * 1280u), rw = mk(p.wqkv, 640u * 3840u), rctl = mk(p.ws, CTL_BYTES);
  unsigned wv[2];
#pragma unroll
  for (int i = 0; i < 2; ++i) {
    const unsigned n = (2 * w + i) * 64 + lane, pos = (n * 2185u) >> 16, cc = n - pos * 30, sec = (cc * 26u) >> 8;
    const unsigned k = (pos & 19) | ((pos & 4) << 1) | ((pos & 8) >> 1);
    wv[i] = k * 3840u + sec * 1120u + cc * 16u;
  }
  const unsigned ws0 = hg * 160u;
  const unsigned xv = (unsigned)(2 * (8 * w + (lane >> 3))) * 1280u + (unsigned)(((lane & 7) ^ (((lane >> 4) & 3) | ((w & 1) << 2))) * 16);
  const unsigned xs0 = (unsigned)(2 * 144 * jt + par) * 1280u;
  const bool isO = job < 20;
  const unsigned lw = lds + w * 1024u, lw2 = lds + w * 2048u;
  Job J; u32x4 Wf[50];
  auto event = [&](auto C) {
    constexpr int c = C.value;
    if constexpr (c >= EV_W) {
      constexpr int s = (c - EV_W) / 4, i = (c - EV_W) % 4;
      if (i == 0 || w < 7) dmai<s * (32u * 3840u), (s & 3) * A_WS + i * 1024u>(rw, wv[i], ws0, lw2);
    } else if constexpr (c >= EV_R) loadW<c - EV_R>(Wf, Q);
    else if constexpr (c >= EV_P) dmai_dev<((c - EV_P) % 5) * 1024u, (c - EV_P) * 8192u>(Q.rq, Q.lv, Q.wo[(c - EV_P) / 5], lw);
    else if constexpr (c == EV_FCOPY) { if (w < 2) copy_wflags(rctl, lds + AX_FW, w, lane); }
    else if constexpr (c >= EV_X) {
      constexpr int s = (c - EV_X) / 2, hf = (c - EV_X) % 2;
      constexpr unsigned so = s * 128u + hf * (160u * 1280u), l = A_XR + (s % 3) * A_XS + hf * 10240u;
      dmai<so, l>(rx, xv, xs0, lw);
      if (w >= 6) dmai<so + 32u * 1280u, l + 2048u>(rx, xv, xs0, lw);
    } else if constexpr (c >= EV_JC && c < EV_JX) { if constexpr (c == EV_JC) job_setup(J, p, b, w, lane); job_loadc<c - EV_JC>(J); }
    else if constexpr (c == EV_JX) job_loadx(J, rx, b, w, lane);
    else if constexpr (c == EV_TDMA) {
      if (job < 74) {
        const rsrc_t rj = mk(sel(isO, p.wo, p.w2), isO ? 640u * 1280u : 1728u * 1280u);
        const int db = 2 * job + (w >> 2), q5 = db / 5, e = db - 5 * q5;
        const int u = !isO ? 32 * (job - 20) + 4 * w : e < 4 ? 20 * (4 * q5 + e) + 4 * (w & 3) : 20 * (4 * q5 + (w & 3)) + 16;
        const unsigned v = g * 1280u + (t ^ (2 * (g | ((w & 1) << 2)))) * 16u;
#pragma unroll
        for (int i = 0; i < 5; ++i) dma(rj, v, u * 1280u + i * 256u, lds + A_JOB + (5 * w + i) * 1024u);
      }
    } else if constexpr (c == EV_GCOPY) {
      if (lane < 40) {
        dma_dev(rctl, Q.lv, WS_GC + (unsigned)((w % 3) * 5376 + (w / 3) * 640 + 80 * hg) * 8u, lds + AX_G + w * 640u);
        if (w == 0) dma_dev(rctl, Q.lv, WS_GC + (unsigned)(2 * 5376 + 2 * 640 + 80 * hg) * 8u, lds + AX_G + 8 * 640u);
      }
      if (w >= 1 && w < 4) dma_dev(rctl, (w < 3 || lane < 32) ? Q.lv : OOB, WS_GR + (unsigned)(par * 1024 + jt * 144 + (w - 1) * 64) * 16u, lds + AX_G + 6144u + (w - 1) * 1024u);
    }
  };
  sstamp(31);
  sfor<15>([&](auto K) { event(IC<ev_at(A_S, -1, 0, K.value)>{}); GAP(); });
  sstamp(32);
  Q.rq = mk(p.ws + WS_W, W_BYTES); Q.lv = lane * 16u;
  unsigned xa[2], wm, wt;
  xa[0] = lds + A_XR + (unsigned)(80 * mg + t) * 128u + (unsigned)((g ^ (t >> 1)) * 16); xa[1] = opaque(xa[0] ^ 64u);
  {
    const int q = t >> 2, pp = t & 3;
    const unsigned rowb = lds + (unsigned)(q + 4 * (g & 1) + 16 * (g >> 1)) * 480u;
    wm = rowb + 40 * h + 8 * pp; wt = rowb + 40 * pp + 32 + h * 160;
  }
  f32x4 acc[4][5];
#pragma unroll
  for (int c = 0; c < 5; ++c) { acc[3][c] = (f32x4){0.f, 0.f, 0.f, 0.f}; pin(acc[3][c]); }
  bf16x8 X[2][5]; u32x2 W[2][8];
  auto rd = [&](auto S, auto R) {
    constexpr int s = S.value, r = R.value;
    if constexpr (r < 5) X[s & 1][r] = ld128(xa[s & 1] + ((s >> 1) % 3) * A_XS + r * 2048u);
    else { constexpr int k = r - 5; W[s & 1][k] = tr64((k < 6 ? wm + (k >> 1) * 160u : wt) + (s & 3) * A_WS + (k & 1) * 3840u); }
  };
  W[0][6] = W[0][7] = W[1][6] = W[1][7] = (u32x2){0u, 0u};
  bool vp[4]; float bias[4], bias0[4];
#pragma unroll
  for (int r = 0; r < 4; ++r) { const int kk = 4 * g + r; vp[r] = kk >= t + 9; const bool vc = kk <= t && kk >= t - 7;
    bias[r] = (vp[r] || vc) ? 0.f : -INFINITY; bias0[r] = ((vp[r] && mg) || vc) ? 0.f : -INFINITY; pin(bias[r]); pin(bias0[r]); }
  unsigned mp0 = (vp[0] ? 0xffffu : 0u) | (vp[1] ? 0xffff0000u : 0u), mp1 = (vp[2] ? 0xffffu : 0u) | (vp[3] ? 0xffff0000u : 0u);
  int dq = 4 * g - t - 9;
  pin(mp0); pin(mp1); pin(dq);
  const int B0 = par * 64 + 9 * jt;
  unsigned ov[4];
#pragma unroll
  for (int ap = 0; ap < 4; ++ap) {
    const int lb = (ap < 3 ? 5 * mg + 2 * ap : 2 * w) + (lane >> 5), B = B0 + lb;
    const bool ok = (lb > 0 || jt == 0) && (ap < 2 || (ap == 2 ? lane < 32 : w < 5));
    ov[ap] = ok ? (unsigned)(B >> 2) * (74u * 4096u) + (B & 3) * 1024u + (lane & 31) * 16u : OOB; pin(ov[ap]);
  }
  unsigned qz = g == h ? lds + 2048u + (unsigned)(5 * mg) * 4096u + t * 32u + g * 8u + 4 * 512u : lds + AX_Z, qs = g == h ? 4096u : 0u;
  pin(qz); pin(qs);
  tag = first_bar<waitA(-1)>(p.ws) + 1;
  stamp(1); cstamp(c0); sstamp(33);
  sfor<11>([&](auto R) { rd(IC<0>{}, R); });
  if (h < 3) { rd(IC<0>{}, IC<11>{}); rd(IC<0>{}, IC<12>{}); }
  sfor<20>([&](auto JC) {
    constexpr int j = JC.value;
    wait_bar<waitA(j)>();
    sstamp(j);
    if constexpr (j == A_FLAG) { if (job < 74 && w == 0 && lane == 0) put_flag(p.ws + WS_FW + job * 128u, tag); }
    constexpr int lead = (j == A_ROWS + 1 || j == A_COLS + 1 || j == J_GRAN + 1 || j == A_TR + 1) ? 1 : 0;
    if constexpr (PRIO && lead) { if (mg) __builtin_amdgcn_s_setprio(1); }
    if constexpr (j == J_ROWS) { land(J.xa); land(J.xb); }
    if constexpr (j == J_COLS) { land(J.c[0]); land(J.c[1]); land(J.c[2]); land(J.c[3]); }
    if constexpr (j == A_CHK) { int w2 = w; asm volatile("" : "+s"(w2)); p2_setup(Q, n2, w2); }
    GAP();
    if (h < 3) {
      if constexpr (j < 19) { rd(IC<j + 1>{}, IC<11>{}); rd(IC<j + 1>{}, IC<12>{}); }
#pragma unroll
      for (int a = 0; a < 5; ++a) mac(acc[3][a], cat(W[j & 1][6], W[j & 1][7]), X[j & 1][a]);
    }
    GAP();
    sfor<15>([&](auto MC) {
      constexpr int m = MC.value, bb = m / 5, a = m % 5;
      if constexpr (j == 0) mac0(acc[bb][a], cat(W[0][2 * bb], W[0][2 * bb + 1]), X[0][a]); else mac(acc[bb][a], cat(W[j & 1][2 * bb], W[j & 1][2 * bb + 1]), X[j & 1][a]);
      if constexpr (j < 19 && m >= ARPOS && m < ARPOS + 11) rd(IC<j + 1>{}, IC<m - ARPOS>{});
      sfor<3>([&](auto K) { event(IC<ev_at(A_S, j, m, K.value)>{}); });
      GAP();
    });
    if constexpr (j == A_TR || j == A_TR + 1) {
      if (mg == j - A_TR && job < 74) {
        const unsigned a = lds + A_JOB + (unsigned)(4 * (g >> 1) + (g & 1)) * 5120u + (t >> 2) * 256u + (t & 3) * 8u, sw = ((t >> 2) | ((g & 1) << 2)) << 5;
        u32x2 TR[10];
#pragma unroll
        for (int m = 0; m < 10; ++m) { const unsigned cb = 160u * w + 32u * (m >> 1); TR[m] = tr64(a + (m & 1) * 10240u + (cb >> 8) * 1024u + ((cb & 255u) ^ sw)); }
#pragma unroll
        for (int cb = 0; cb < 5; ++cb)
          st128_dev((u32x4){TR[2 * cb].x, TR[2 * cb].y, TR[2 * cb + 1].x, TR[2 * cb + 1].y}, Q.rq, Q.lv, (unsigned)(w * 74 + job) * 5120u + cb * 1024u);
      }
    }
    if constexpr (j == A_ROWS || j == A_ROWS + 1) { if (mg == j - A_ROWS) job_rows(J, rctl, b, w, lane, (unsigned)tag); }
    if constexpr (j == A_COLS || j == A_COLS + 1) { if (mg == j - A_COLS) job_cols(J, lds + AX_JP, w, lane); }
    if constexpr (j == A_COLS + 2) { if (w == 1) job_pubc(lds + AX_JP, rctl, b, lane, (unsigned)tag); }
    if constexpr (j == A_CHK) { if (w == 3) check_wflags(p, lds + AX_FW, lane, tag); }
    if constexpr (j == J_GRAN) {
      if (w < 4) {
        const int tid = w * 64 + lane, c = tid < 240 ? tid : 0, sec = c / 80, cc = c - 80 * sec;
        *(LDSP float*)(size_t)(lds + AX_CS + tid * 4u) = colsum(lds + AX_G + sec * 1920u + cc * 8u, 640u, p.ws, sec * 640 + 80 * hg + cc, (unsigned)tag);
      }
    }
    if constexpr (j == J_GRAN + 1) {
      if (w == 7) *(LDSP u32x2*)(size_t)(lds + AX_Z) = (u32x2){0u, 0u};
      else if (w >= 4) {
        const int r0 = (w - 4) * 64 + lane, r = r0 < 160 ? r0 : 0;
        *(LDSP f32x2*)(size_t)(lds + AX_ST + r0 * 8u) = rowstat(lds + AX_G + 6144u + r * 16u, p.ws, par * 1024 + jt * 144 + r, (unsigned)tag);
      }
    }
    if constexpr (PRIO && lead) __builtin_amdgcn_s_setprio(0);
    GAP();
  });
  settle();
  stamp(2); cstamp(c1); sstamp(20);
  constexpr unsigned E_KB = 0, E_X = 2048, E_OT = E_X + 40960;
  auto pre = [&](auto K) { loadW<25 + K.value>(Wf, Q); };
  u32x4 QT[5], KT[6];
  {
    f32x4 c4[4]; f32x2 stt[5];
#pragma unroll
    for (int bb = 0; bb < 3; ++bb) c4[bb] = ld128f(lds + AX_CS + (bb * 80 + 20 * h + 4 * g) * 4u);
    c4[3] = ld128f(lds + AX_CS + ((h < 3 ? h : 0) * 80 + 20 * g + 16) * 4u);
#pragma unroll
    for (int a = 0; a < 5; ++a) stt[a] = ld64f(lds + AX_ST + (80 * mg + 16 * a + t) * 8u);
    const unsigned xw = lds + E_X + (unsigned)(5 * mg) * 4096u + h * 512u + t * 32u + g * 8u;
    sfor<5>([&](auto A) {
      constexpr int a = A.value;
      const f32x2 nm = bc2(-stt[a].x), rs = bc2(stt[a].y);
      f32x2 v[8];
#pragma unroll
      for (int bb = 0; bb < 4; ++bb) { v[2 * bb] = fma2(nm, lo2(c4[bb]), lo2(acc[bb][a])); v[2 * bb + 1] = fma2(nm, hi2(c4[bb]), hi2(acc[bb][a])); }
      pre(IC<2 * a>{}); GAP();
#pragma unroll
      for (int k = 0; k < 8; ++k) v[k] = v[k] * rs;
      pre(IC<2 * a + 1>{}); GAP();
      QT[a].x = pk2(v[0]); QT[a].y = pk2(v[1]); KT[a + 1].x = pk2(v[2]); KT[a + 1].y = pk2(v[3]);
      *(LDSP u32x2*)(size_t)(xw + a * 4096u) = (u32x2){pk2(v[4]), pk2(v[5])};
      *(LDSP u32x2*)(size_t)(xw + a * 4096u + 2048u) = (u32x2){pk2(v[6]), pk2(v[7])};
      { unsigned q0 = QT[a].x, q1 = QT[a].y, k0 = KT[a + 1].x, k1 = KT[a + 1].y; pin(q0); pin(q1); pin(k0); pin(k1); QT[a].x = q0; QT[a].y = q1; KT[a + 1].x = k0; KT[a + 1].y = k1; }
      GAP();
    });
    *(LDSP u32x2*)(size_t)(lds + E_KB + (mg ? 4096u + 43008u : 0u) + h * 512u + lane * 8u) = (u32x2){KT[5].x, KT[5].y};
  }
  sstamp(21);
  lds_bar();
  sstamp(22);
  const rsrc_t rh = mk(p.ws + WS_H, H_BYTES);
  {
    const unsigned xk = opaque(lds + E_X + (unsigned)(5 * mg) * 4096u + t * 32u + g * 8u + 5 * 512u);
    const unsigned xt = opaque(lds + E_X + (unsigned)(5 * mg) * 4096u + (unsigned)(4 * g + (t >> 2)) * 32u + (t & 3) * 8u + 6 * 512u);
    const unsigned xv2 = opaque(xt + h * 512u - 6 * 512u);
    const unsigned xv3 = opaque(xv2), xt3 = opaque(xt);
    const unsigned pb = mg ? 4096u : 0u;
    u32x4 VV[5], TT[5];
    { const u32x2 k0 = ld64(lds + E_KB + h * 512u + lane * 8u), k1 = ld64(xk - pb); KT[0] = (u32x4){k0.x, k0.y, k1.x, k1.y}; }
#pragma unroll
    for (int a = 0; a < 5; ++a) {
      const u32x2 q = ld64v(qz + a * qs), k = ld64v(xk + a * 4096u);
      QT[a].z = q.x; QT[a].w = q.y; KT[a + 1].z = k.x; KT[a + 1].w = k.y;
    }
#pragma unroll
    for (int a = 0; a < 5; ++a) {
      const u32x2 v0 = tr64(a ? xv2 + (a - 1) * 4096u : xv2 - pb), v1 = tr64(xv3 + a * 4096u), t0 = tr64(a ? xt + (a - 1) * 4096u : xt - pb), t1 = tr64(xt3 + a * 4096u);
      VV[a] = (u32x4){v0.x, v0.y, v1.x, v1.y}; TT[a] = (u32x4){t0.x, t0.y, t1.x, t1.y};
    }  GAP();
    const float c = 0.22360679775f * 1.44269504089f;
    const f32x4 z = {0.f, 0.f, 0.f, 0.f};
    f32x4 sp[5], sc[5];
#pragma unroll
    for (int a = 0; a < 5; ++a) { sp[a] = mfma(asb(KT[a]), asb(QT[a]), z); sc[a] = mfma(asb(KT[a + 1]), asb(QT[a]), z); }  pre(IC<10>{}); GAP();
    float s[5][4], mx[5], my[5];
    pin(dq);
#pragma unroll
    for (int r = 0; r < 4; ++r) vp[r] = dq >= -r;
#pragma unroll
    for (int a = 0; a < 5; ++a)
#pragma unroll
      for (int r = 0; r < 4; ++r) s[a][r] = vp[r] ? sp[a][r] : sc[a][r];  pre(IC<11>{}); GAP();
#pragma unroll
    for (int a = 0; a < 5; ++a)
#pragma unroll
      for (int r = 0; r < 4; ++r) s[a][r] = fmaf(s[a][r], c, a == 0 ? bias0[r] : bias[r]);  pre(IC<12>{}); GAP();
#pragma unroll
    for (int a = 0; a < 5; ++a) mx[a] = max3(s[a][0], s[a][1], s[a][2]);
    GAP();
#pragma unroll
    for (int a = 0; a < 5; ++a) mx[a] = max2(mx[a], s[a][3]);
    pre(IC<13>{}); GAP();
#pragma unroll
    for (int a = 0; a < 5; ++a) { auto r = __builtin_amdgcn_permlane16_swap(asu(mx[a]), asu(mx[a]), false, false); unsigned r0 = r[0], r1 = r[1]; mx[a] = asf(r0); my[a] = asf(r1); }  GAP();
#pragma unroll
    for (int a = 0; a < 5; ++a) mx[a] = max2(mx[a], my[a]);
    pre(IC<14>{}); GAP();
#pragma unroll
    for (int a = 0; a < 5; ++a) { auto r = __builtin_amdgcn_permlane32_swap(asu(mx[a]), asu(mx[a]), false, false); unsigned r0 = r[0], r1 = r[1]; mx[a] = asf(r0); my[a] = asf(r1); }
    GAP();
#pragma unroll
    for (int a = 0; a < 5; ++a) mx[a] = max2(mx[a], my[a]);  pre(IC<15>{}); GAP();
#pragma unroll
    for (int a = 0; a < 5; ++a)
#pragma unroll
      for (int r = 0; r < 4; ++r) s[a][r] -= mx[a];  pre(IC<16>{}); GAP();
#pragma unroll
    for (int a = 0; a < 5; ++a)
#pragma unroll
      for (int r = 0; r < 4; ++r) s[a][r] = __builtin_amdgcn_exp2f(s[a][r]);   pre(IC<17>{}); GAP();
    unsigned p0[5], p1[5];
#pragma unroll
    for (int a = 0; a < 5; ++a) { p0[a] = pk(s[a][0], s[a][1]); p1[a] = pk(s[a][2], s[a][3]); }  pre(IC<18>{}); GAP();
    f32x4 om[5], ot[5], ol[5];
    const bf16x8 ones = asb((u32x4){0x3f803f80u, 0x3f803f80u, 0x3f803f80u, 0x3f803f80u});
#pragma unroll
    for (int a = 0; a < 5; ++a) {
      const bf16x8 pf = asb((u32x4){p0[a] & mp0, p1[a] & mp1, p0[a] & ~mp0, p1[a] & ~mp1});
      ol[a] = mfma(ones, pf, z); om[a] = mfma(asb(VV[a]), pf, z); ot[a] = mfma(asb(TT[a]), pf, z);
    }  pre(IC<19>{}); GAP();
    float il[5];
#pragma unroll
    for (int a = 0; a < 5; ++a) il[a] = __builtin_amdgcn_rcpf(ol[a].x);
    GAP();
    f32x2 o2[5][4];
#pragma unroll
    for (int a = 0; a < 5; ++a) { const f32x2 i2 = bc2(il[a]); o2[a][0] = lo2(om[a]) * i2; o2[a][1] = hi2(om[a]) * i2; o2[a][2] = lo2(ot[a]) * i2; o2[a][3] = hi2(ot[a]) * i2; }  GAP();
    u32x2 O[6], Tl[5];
#pragma unroll
    for (int a = 0; a < 5; ++a) { O[a] = (u32x2){pk2(o2[a][0]), pk2(o2[a][1])}; Tl[a] = (u32x2){pk2(o2[a][2]), pk2(o2[a][3])}; }
    GAP();
    if (g == h) {
#pragma unroll
      for (int a = 0; a < 5; ++a) *(LDSP u32x2*)(size_t)(lds + E_OT + (5 * mg + a) * 512u + ((h & 1) * 16 + t) * 16u + (h >> 1) * 8u) = Tl[a];
    }
    O[5] = O[4];
    const int db = 5 * hg + h;
    const unsigned so = (db >> 1) * 4096u + (db & 1) * 512u;
#pragma unroll
    for (int ap = 0; ap < 3; ++ap) st128_dev(halffrag(O[2 * ap], O[2 * ap + 1]), rh, ov[ap], so);
  }
  sstamp(23);
  lds_bar();
  sstamp(24);
  {
    const int db = 5 * hg + 4;
    st128_dev(ld128u(lds + E_OT + (w < 5 ? w : 0) * 1024u + lane * 16u), rh, ov[3], (db >> 1) * 4096u + (db & 1) * 512u);
  }
  sstamp(25);
  wait_bar<0>();
  sstamp(26); sstamp(27); sstamp(28);
  if (w == 0 && lane == 0) put_flag(p.ws + WS_FH + myflag * 64u, tag);
  sfor<6>([&](auto R) { dmai_dev<(R.value % 5) * 1024u, R.value * 8192u>(Q.rq, Q.lv, Q.wo[R.value / 5], lw); });
  dmai_dev<3072u, 18 * 8192u>(Q.rq, Q.lv, Q.wo[3], lw); dmai_dev<4096u, 19 * 8192u>(Q.rq, Q.lv, Q.wo[3], lw);
  out_tile<9, 9>(p, ST, Q, Wf, mg, lds, w, lane, mt, n2, tag);
}

__global__ __launch_bounds__(512, 2) void fused_block(Params p0) {
  Stamps ST;
  stamp(9);
  sstamp(29);
  const int lane = threadIdx.x & 63;
  const int b = blockIdx.x, xcd = b & 7, i = b >> 3, par = xcd & 1, cgp = xcd >> 1;
  {
    u64 pc; asm volatile("s_getpc_b64 %0" : "=s"(pc));
    const unsigned o = min((unsigned)(i * 16 + (lane & 15)) * 128u, (unsigned)CODE_KB * 1024u);
    asm volatile("s_mov_b32 m0, %2\n\ts_nop 0\n\tglobal_load_lds_dword %0, %1" :: "v"(o), "s"(pc), "i"(LDS_BYTES - 256) : "memory");
  }
  Params p = p0;
  asm volatile("" : "+s"(p.x), "+s"(p.wqkv), "+s"(p.wo), "+s"(p.w1), "+s"(p.w3), "+s"(p.w2), "+s"(p.out), "+s"(p.ws));
  extern __shared__ __attribute__((aligned(16))) char smem[];
  const unsigned lds = (unsigned)(size_t)(LDSP char*)smem;
  const int w = rfl(threadIdx.x >> 6);
  sstamp(30);
  stamp(0);
  u64 tag;
  const int mt = xcd * 4 + (i >> 3), n2 = i & 7;
  if (i < 18) { const int u = 18 * cgp + i; ffn_tile(p, ST, lds, b, w, lane, par, u & 7, u >> 3, mt, n2, (par * 8 + (u & 7)) * 9 + (u >> 3), tag); }
  else { const int a = i - 18, hi = a >= 7, jt = a - 7 * hi, hg = 2 * cgp + hi; att_tile(p, ST, lds, b, w, lane, par, jt, hg, a * 8 + xcd, mt, n2, 144 + (par * 7 + jt) * 8 + hg, tag); }
}

extern "C" void kernel_launch(void* const* d_in, const int* in_sizes, int n_in, void* d_out, int out_size, void* d_ws, size_t ws_size, hipStream_t stream) {
  Params p;
  p.x = (const char*)d_in[0]; p.wqkv = (const char*)d_in[1]; p.wo = (const char*)d_in[2];
  p.w1 = (const char*)d_in[4]; p.w3 = (const char*)d_in[5]; p.w2 = (const char*)d_in[6];
  p.out = (char*)d_out; p.ws = (char*)d_ws;
  (void)hipFuncSetAttribute((const void*)fused_block, hipFuncAttributeMaxDynamicSharedMemorySize, LDS_BYTES);
  fused_block<<<256, 512, LDS_BYTES, stream>>>(p);
}
```

```cpp
#include <hip/hip_runtime.h>
#include <hip/hip_bf16.h>
#include <utility>

typedef short    s16x4 __attribute__((ext_vector_type(4)));
typedef __bf16   bf16x2 __attribute__((ext_vector_type(2)));
typedef __bf16   bf16x8 __attribute__((ext_vector_type(8)));
typedef float    f32x2 __attribute__((ext_vector_type(2)));
typedef float    f32x4 __attribute__((ext_vector_type(4)));
typedef unsigned u32x2 __attribute__((ext_vector_type(2)));
typedef unsigned u32x4 __attribute__((ext_vector_type(4)));
typedef unsigned long long u64;
typedef __amdgpu_buffer_rsrc_t rsrc_t;
#define LDSP __attribute__((address_space(3)))
#define GAS __attribute__((address_space(1)))
#define DI __device__ __forceinline__
#define GAP() __builtin_amdgcn_sched_barrier(0)
template <int V> using IC = std::integral_constant<int, V>;
template <class F, int... I> DI void sfor_(F&& f, std::integer_sequence<int, I...>) { (f(IC<I>{}), ...); }
template <int N, class F> DI void sfor(F&& f) { sfor_(f, std::make_integer_sequence<int, N>{}); }

#define STAMPS 0
#define ROT 0
#define DPOS 16
#define RPOS 0
#define ADPOS 11
#define ARPOS 0
#define NTOUCH 0
#define LTOUCH 0
#define JEARLY 1
#define NPRE 16
#define PORD 2
#define BAL 0
#define FCHK 14
#define NINL 10
#define NOLD 1
#define MIDST 1
#define KEEPW 8
#define APRE 12
#define PRIO 1
#define ABL 0

constexpr unsigned WS_E  = 0;
constexpr unsigned WS_FH = 4096;
constexpr unsigned WS_FW = 20480;
constexpr unsigned WS_GR = 32768;
constexpr unsigned WS_GC = 65536;
constexpr unsigned CTL_BYTES = WS_GC + 3u * 5376u * 8u;
constexpr unsigned WS_H = 1u << 20;
constexpr unsigned H_BYTES = 32u * 74u * 4096u;
constexpr unsigned WS_W = 16u << 20;
constexpr unsigned W_BYTES = 8u * 74u * 5120u;
constexpr unsigned WS_ST = 32u << 20;
constexpr unsigned OOB = 0x7f000000u;
constexpr int LDS_BYTES = 163840;
constexpr int SC1 = 16;
constexpr int SPIN_CAP = 1 << 18;
constexpr int CODE_KB = 44;

struct Params {
  const char* x; const char* wqkv; const char* wo; const char* w1; const char* w3; const char* w2;
  char* out; char* ws;
};

DI const char* sel(bool c, const char* a, const char* b) { return c ? a : b; }
DI unsigned rfl(unsigned v) { return __builtin_amdgcn_readfirstlane(v); }
DI rsrc_t mk(const void* p, unsigned bytes) { return __builtin_amdgcn_make_buffer_rsrc((void*)p, (short)0, (int)bytes, 0x00020000); }
DI void dma(rsrc_t r, unsigned voff, unsigned soff, unsigned lds) {
  asm volatile("s_mov_b32 m0, %0\n\ts_nop 0\n\tbuffer_load_dwordx4 %1, %2, %3 offen lds" :: "s"(lds), "v"(voff), "s"(r), "s"(soff) : "memory");
}
DI void dma_dev(rsrc_t r, unsigned voff, unsigned soff, unsigned lds) {
  asm volatile("s_mov_b32 m0, %0\n\ts_nop 0\n\tbuffer_load_dwordx4 %1, %2, %3 offen sc1 lds" :: "s"(lds), "v"(voff), "s"(r), "s"(soff) : "memory");
}
template <unsigned SI, unsigned LI> DI void dmai(rsrc_t r, unsigned voff, unsigned soff, unsigned lds) {
  unsigned t;
  asm volatile("s_add_u32 m0, %1, %5\n\ts_add_u32 %0, %4, %6\n\tbuffer_load_dwordx4 %2, %3, %0 offen lds" : "=&s"(t) : "s"(lds), "v"(voff), "s"(r), "s"(soff), "i"(LI), "i"(SI) : "memory", "scc");
}
template <unsigned SI, unsigned LI> DI void dmai_dev(rsrc_t r, unsigned voff, unsigned soff, unsigned lds) {
  unsigned t;
  asm volatile("s_add_u32 m0, %1, %5\n\ts_add_u32 %0, %4, %6\n\tbuffer_load_dwordx4 %2, %3, %0 offen sc1 lds" : "=&s"(t) : "s"(lds), "v"(voff), "s"(r), "s"(soff), "i"(LI), "i"(SI) : "memory", "scc");
}
DI void st128_dev(u32x4 d, rsrc_t r, unsigned voff, unsigned soff) {
  asm volatile("buffer_store_dwordx4 %0, %1, %2, %3 offen sc1\n\ts_nop 1" :: "v"(d), "v"(voff), "s"(r), "s"(soff) : "memory");
}
DI u32x4 hload4(rsrc_t r, unsigned voff, unsigned soff) { u32x4 v; asm volatile("buffer_load_dwordx4 %0, %1, %2, %3 offen" : "=v"(v) : "v"(voff), "s"(r), "s"(soff) : "memory"); return v; }
DI unsigned hload1(rsrc_t r, unsigned voff, unsigned soff) { unsigned v; asm volatile("buffer_load_dword %0, %1, %2, %3 offen" : "=v"(v) : "v"(voff), "s"(r), "s"(soff) : "memory"); return v; }
template <class T> DI void land(T& v) { asm volatile("" : "+v"(v) :: "memory"); }
template <int N> DI void wait_bar() { asm volatile("s_waitcnt vmcnt(%0) lgkmcnt(0)\n\ts_barrier" :: "i"(N) : "memory"); }
template <int N> DI u64 first_bar(const char* e) {
  u64 v; asm volatile("s_load_dwordx2 %0, %1, 0x0\n\ts_waitcnt vmcnt(%2) lgkmcnt(0)\n\ts_barrier" : "=&s"(v) : "s"(e), "i"(N) : "memory"); return v;
}
DI void lds_bar() { asm volatile("s_waitcnt lgkmcnt(0)\n\ts_barrier" ::: "memory"); }
DI unsigned opaque(unsigned v) { asm volatile("" : "+v"(v)); return v; }
DI bf16x8 ld128(unsigned a) { return *(const LDSP bf16x8*)(size_t)a; }
DI u32x4  ld128u(unsigned a) { return *(const LDSP u32x4*)(size_t)a; }
DI f32x4  ld128f(unsigned a) { return *(const LDSP f32x4*)(size_t)a; }
DI u32x2  ld64(unsigned a) { return *(const LDSP u32x2*)(size_t)a; }
DI u32x2  ld64v(unsigned a) { return *(const volatile LDSP u32x2*)(size_t)a; }
DI f32x2  ld64f(unsigned a) { return *(const LDSP f32x2*)(size_t)a; }
DI float  ld32f(unsigned a) { return *(const LDSP float*)(size_t)a; }
DI u32x2  tr64(unsigned a) { s16x4 v = __builtin_amdgcn_ds_read_tr16_b64_v4i16((LDSP s16x4*)(size_t)a); return __builtin_bit_cast(u32x2, v); }
DI bf16x8 cat(u32x2 a, u32x2 b) { u32x4 v = {a.x, a.y, b.x, b.y}; return __builtin_bit_cast(bf16x8, v); }
DI bf16x8 asb(u32x4 v) { return __builtin_bit_cast(bf16x8, v); }
DI f32x4 mfma(bf16x8 a, bf16x8 b, f32x4 c) { return __builtin_amdgcn_mfma_f32_16x16x32_bf16(a, b, c, 0, 0, 0); }
DI void mac(f32x4& c, bf16x8 a, bf16x8 b) { asm volatile("v_mfma_f32_16x16x32_bf16 %0, %1, %2, %0" : "+v"(c) : "v"(a), "v"(b)); }
DI void mac0(f32x4& c, bf16x8 a, bf16x8 b) { asm volatile("v_mfma_f32_16x16x32_bf16 %0, %1, %2, 0" : "=&v"(c) : "v"(a), "v"(b)); }
DI void settle() { asm volatile("s_nop 7\n\ts_nop 1" ::: "memory"); GAP(); }
DI unsigned pk(float lo, float hi) { f32x2 v = {lo, hi}; bf16x2 b = __builtin_convertvector(v, bf16x2); return __builtin_bit_cast(unsigned, b); }
DI float asf(unsigned u) { return __builtin_bit_cast(float, u); }
DI unsigned asu(float f) { return __builtin_bit_cast(unsigned, f); }
DI f32x2 fma2(f32x2 a, f32x2 b, f32x2 c) { return __builtin_elementwise_fma(a, b, c); }
DI f32x2 lo2(f32x4 v) { return __builtin_shufflevector(v, v, 0, 1); }
DI f32x2 hi2(f32x4 v) { return __builtin_shufflevector(v, v, 2, 3); }
DI f32x2 bc2(float v) { return (f32x2){v, v}; }
DI unsigned pk2(f32x2 v) { bf16x2 b = __builtin_convertvector(v, bf16x2); return __builtin_bit_cast(unsigned, b); }
DI float max3(float a, float b, float c) { float r; asm("v_max3_f32 %0, %1, %2, %3" : "=v"(r) : "v"(a), "v"(b), "v"(c)); return r; }
DI float max2(float a, float b) { float r; asm("v_max_f32 %0, %1, %2" : "=v"(r) : "v"(a), "v"(b)); return r; }
DI float blo(unsigned u) { return asf(u << 16); }
DI float bhi(unsigned u) { return asf(u & 0xffff0000u); }
DI float swadd32(float a, float b) { auto r = __builtin_amdgcn_permlane32_swap(asu(a), asu(b), false, false); unsigned r0 = r[0], r1 = r[1]; return asf(r0) + asf(r1); }
DI float swadd16(float a, float b) { auto r = __builtin_amdgcn_permlane16_swap(asu(a), asu(b), false, false); unsigned r0 = r[0], r1 = r[1]; return asf(r0) + asf(r1); }
template <int C> DI float dppadd(float v) { return v + asf(__builtin_amdgcn_update_dpp(0u, asu(v), C, 0xf, 0xf, true)); }
DI float rsum4(float v) { v = swadd16(v, v); return swadd32(v, v); }
DI float wsum(float v) { v = dppadd<0xB1>(v); v = dppadd<0x4E>(v); v = dppadd<0x141>(v); v = dppadd<0x128>(v); return rsum4(v); }
DI float rmax4(float v) {
  auto a = __builtin_amdgcn_permlane16_swap(asu(v), asu(v), false, false); unsigned a0 = a[0], a1 = a[1]; v = max2(asf(a0), asf(a1));
  auto b = __builtin_amdgcn_permlane32_swap(asu(v), asu(v), false, false); unsigned b0 = b[0], b1 = b[1]; return max2(asf(b0), asf(b1));
}
DI u32x4 halffrag(u32x2 a, u32x2 b) {
  auto p = __builtin_amdgcn_permlane32_swap(a.x, b.x, false, false);
  auto q = __builtin_amdgcn_permlane32_swap(a.y, b.y, false, false);
  unsigned p0 = p[0], p1 = p[1], q0 = q[0], q1 = q[1];
  return (u32x4){p0, q0, p1, q1};
}
DI void poll(const char* addr, u64 tag) {
#pragma unroll 1
  for (int i = 0; i < SPIN_CAP; ++i) {
    u32x2 v; asm volatile("global_load_dwordx2 %0, %1, off sc1\n\ts_waitcnt vmcnt(0)" : "=&v"(v) : "v"(addr) : "memory");
    const unsigned lo = v.x, hi = v.y;
    if (__all(lo == (unsigned)tag && hi == (unsigned)(tag >> 32))) break;
    __builtin_amdgcn_s_sleep(1);
  }
}
DI void put_flag(char* addr, u64 tag) { __hip_atomic_store((GAS u64*)addr, tag, __ATOMIC_RELAXED, __HIP_MEMORY_SCOPE_AGENT); }
struct Stamps {};
#define stamp(slot) do {} while (0)
#define cstamp(f) do {} while (0)
#define dump_sv() do {} while (0)
#define sstamp(j) do {} while (0)

struct Job {
  rsrc_t rc; unsigned cv, cstride;
  u32x4 c[4], xa; unsigned xb;
};
DI void job_setup(Job& J, const Params& p, int b, int w, int lane) {
  const int grp = b / 3, th = b - 3 * grp;
  const bool q = grp < 30, one = grp < 57;
  J.cstride = q ? 3840u : 3456u;
  J.rc = mk(sel(q, p.wqkv, sel(one, p.w1, p.w3)), grp < 84 ? 640u * J.cstride : 0u);
  J.cv = __umul24(8 * (3 * w + th) + (lane >> 3), J.cstride) + (unsigned)((q ? grp : one ? grp - 30 : grp - 57) * 128 + (lane & 7) * 16);
}
template <int I> DI void job_loadc(Job& J) { J.c[I] = hload4(J.rc, J.cv, I * 192u * J.cstride); }
DI void job_loadx(Job& J, rsrc_t rx, int b, int w, int lane) {
  const int v = 8 * b + w; const unsigned so = (unsigned)(2 * (v & 1023) + (v >> 10)) * 1280u;
  J.xa = hload4(rx, lane * 16u, so); J.xb = hload1(rx, 1024u + lane * 4u, so);
}
template <class T> DI void pin(T& v) { asm volatile("" : "+v"(v)); }
DI void job_cols(Job& J, unsigned part  , int w, int lane) {
  const int t = lane & 15; const unsigned one = (t & 1) ? 0x3f800000u : 0x00003f80u;
  const bf16x8 sel8 = asb((u32x4){(t >> 1) == 0 ? one : 0u, (t >> 1) == 1 ? one : 0u, (t >> 1) == 2 ? one : 0u, (t >> 1) == 3 ? one : 0u});
  f32x4 d = {0.f, 0.f, 0.f, 0.f};
#pragma unroll
  for (int i = 0; i < 4; ++i) d = mfma(sel8, asb(J.c[i]), d);
  const f32x4 y = {dppadd<0x128>(d.x), dppadd<0x128>(d.y), dppadd<0x128>(d.z), dppadd<0x128>(d.w)};
  *(LDSP f32x4*)(size_t)(part + (lane < 32 ? w * 256u : 2048u) + (lane & 7) * 32u + (lane & 16)) = y;
}
DI float dot2(unsigned a, unsigned b, float c) { return __builtin_amdgcn_fdot2_f32_bf16(__builtin_bit_cast(bf16x2, a), __builtin_bit_cast(bf16x2, b), c, false); }
DI void job_rows(Job& J, rsrc_t rctl, int b, int w, int lane, unsigned tag) {
  const unsigned a0 = J.xa.x, a1 = J.xa.y, a2 = J.xa.z, a3 = J.xa.w, a4 = J.xb, o = 0x3f803f80u;
  const float s = dot2(a4, o, dot2(a3, o, dot2(a2, o, dot2(a1, o, dot2(a0, o, 0.f)))));
  const float q = dot2(a4, a4, dot2(a3, a3, dot2(a2, a2, dot2(a1, a1, dot2(a0, a0, 0.f)))));
  float u = swadd32(s, q);
  u = dppadd<0xB1>(u); u = dppadd<0x4E>(u); u = dppadd<0x141>(u); u = dppadd<0x128>(u); u = swadd16(u, u);
  __builtin_amdgcn_raw_buffer_store_b64((u32x2){asu(u), tag}, rctl, (lane & 31) ? OOB : lane >> 2, WS_GR + (8 * b + w) * 16u, SC1);
}
DI u32x2 regrab(const char* src, unsigned tag) {
  u32x2 v = {0u, 0u};
#pragma unroll 1
  for (int i = 0; i < SPIN_CAP; ++i) {
    asm volatile("global_load_dwordx2 %0, %1, off sc1\n\ts_waitcnt vmcnt(0)" : "=&v"(v) : "v"(src) : "memory");
    const unsigned tg = v.y;
    if (__all(tg == tag)) break;
    __builtin_amdgcn_s_sleep(1);
  }
  return v;
}
DI float colsum(unsigned a, unsigned st, const char* ws, unsigned col, unsigned tag) {
  u32x2 g0 = ld64(a), g1 = ld64(a + st), g2 = ld64(a + 2 * st);
  const unsigned t0 = g0.y, t1 = g1.y, t2 = g2.y;
  if (!__all(t0 == tag && t1 == tag && t2 == tag)) { const char* src = ws + WS_GC + col * 8u; g0 = regrab(src, tag); g1 = regrab(src + 5376 * 8, tag); g2 = regrab(src + 2 * 5376 * 8, tag); }
  const unsigned v0 = g0.x, v1 = g1.x, v2 = g2.x;
  return asf(v0) + asf(v1) + asf(v2);
}
DI f32x2 rowstat(unsigned a, const char* ws, unsigned row, unsigned tag) {
  const u32x4 v = ld128u(a);
  unsigned s = v.x, q = v.z; const unsigned t0 = v.y, t1 = v.w;
  if (!__all(t0 == tag && t1 == tag)) { const char* src = ws + WS_GR + row * 16u; const u32x2 g0 = regrab(src, tag), g1 = regrab(src + 8, tag); s = g0.x; q = g1.x; }
  const float m = asf(s) * (1.f / 640.f);
  return (f32x2){m, __builtin_amdgcn_rsqf(fmaxf(fmaf(asf(q), 1.f / 640.f, -m * m), 0.f) + 1e-5f)};
}
DI void copy_wflags(rsrc_t rctl, unsigned dst, int half, int lane) { dma_dev(rctl, (half == 0 || lane < 10) ? lane * 128u : OOB, WS_FW + half * 8192u, dst + half * 1024u); }
DI void check_wflags(const Params& p, unsigned src, int lane, u64 tag) {
  const u32x2 f = ld64(src + lane * 16u), e = ld64(src + (lane < 10 ? 1024u + lane * 16u : 0u));
  const unsigned f0 = f.x, f1 = f.y, e0 = e.x, e1 = e.y, lo = (unsigned)tag, hi = (unsigned)(tag >> 32);
  if (!__all(f0 == lo && f1 == hi && e0 == lo && e1 == hi)) { poll(p.ws + WS_FW + lane * 128u, tag); poll(p.ws + WS_FW + (lane < 10 ? 64 + lane : 0) * 128u, tag); }
}
DI void job_pubc(unsigned part, rsrc_t rctl, int b, int lane, unsigned tag) {
  float s = 0.f;
#pragma unroll
  for (int k = 0; k < 8; ++k) s += ld32f(part + k * 256u + lane * 4u);
  const int grp = b / 3, th = b - 3 * grp;
  __builtin_amdgcn_raw_buffer_store_b64((u32x2){asu(s), tag}, rctl, grp < 84 ? lane * 8u : OOB, WS_GC + (unsigned)(th * 5376 + grp * 64) * 8u, SC1);
}

struct P2 { rsrc_t rq; unsigned st[10], wo[10], lv; };
DI void p2_setup(P2& Q, int n, int w) {
  const int hs = w < 2 ? 20 + 7 * w : w < 4 ? 22 + 6 * w : 18 + 7 * w;
#pragma unroll
  for (int s = 0; s < 6; ++s) Q.st[s] = hs + s;
  Q.st[6] = (w & 6) == 2 ? 16 + w : hs + 6; Q.st[7] = 2 * w; Q.st[8] = 2 * w + 1;
  Q.st[9] = w < 2 ? 16 + w : 0x10000;
#pragma unroll
  for (int s = 0; s < 10; ++s) Q.wo[s] = (n * 74u + Q.st[s]) * 5120u;
}

enum { EV_NONE = 0, EV_W = 1000  , EV_X = 300  , EV_P = 500  ,
       EV_JC = 10  , EV_JX = 14, EV_FCOPY = 16, EV_GCOPY = 17, EV_TDMA = 18, EV_TST = 19, EV_R = 600   };
struct Sched { int n; int j[300], m[300], c[300]; };
constexpr int J_ROWS = PORD >= 6 ? 3 : PORD ? 2 : JEARLY ? 3 : 6, J_COLS = PORD >= 6 ? 5 : PORD ? 4 : JEARLY ? 8 : 9, J_PUB = J_COLS + 2, J_GRAN = 17, F_CHK = FCHK, A_TR = 4, A_FLAG = 8, A_ROWS = PORD >= 6 ? 8 : PORD ? 2 : 6, A_COLS = PORD ? 6 : J_COLS  , A_CHK = 14;
constexpr Sched make_sched(bool ffn) {
  Sched S{}; int n = 0;
  const int NW = ffn ? 3 : 2;
  auto put = [&](int j, int m, int c) { S.j[n] = j; S.m[n] = m; S.c[n] = c; ++n; };
  for (int j = -1; j < 20; ++j) {
    int L[24] = {}; int k = 0;
    auto W = [&](int s) { for (int i = 0; i < NW; ++i) { if (s < 20) L[k++] = EV_W + 4 * s + i; else if (ffn) L[k++] = EV_P + (s & 3) * 3 + i; } };
    auto X = [&](int s, int h) { if (s < 10) L[k++] = EV_X + 2 * s + h; else if (ffn) L[k++] = EV_P + 12 + (s % 3) * 2 + h; };
    auto E = [&](int c) { L[k++] = c; };
    if (j == -1) {
      if (PORD == 0) { W(0); X(0, 0); X(0, 1); W(1); W(2); X(1, 0); X(1, 1); }
      if (PORD == 1) { E(EV_JC); W(0); X(0, 0); X(0, 1); E(EV_JX); W(1); E(EV_JC + 1); W(2); X(1, 0); X(1, 1); E(EV_JC + 2); E(EV_JC + 3); }
      if (PORD == 2) { W(0); X(0, 0); X(0, 1); E(EV_JC); E(EV_JX); W(1); E(EV_JC + 1); W(2); X(1, 0); X(1, 1); E(EV_JC + 2); E(EV_JC + 3); }
      if (PORD == 3) { E(EV_JC); E(EV_JX); E(EV_JC + 1); E(EV_JC + 2); E(EV_JC + 3); W(0); X(0, 0); X(0, 1); W(1); W(2); X(1, 0); X(1, 1); }
      if (PORD == 4) { E(EV_JC); W(0); X(0, 0); X(0, 1); W(1); E(EV_JX); E(EV_JC + 1); E(EV_JC + 2); E(EV_JC + 3); W(2); X(1, 0); X(1, 1); }
      if (PORD == 5) { W(0); X(0, 0); X(0, 1); W(1); W(2); X(1, 0); X(1, 1); E(EV_JC); E(EV_JX); E(EV_JC + 1); E(EV_JC + 2); E(EV_JC + 3); }
      if (PORD == 6) { W(0); X(0, 0); X(0, 1); E(EV_JC); W(1); }
      if (PORD == 7) { W(0); X(0, 0); X(0, 1); W(1); E(EV_JC); }
      if (PORD == 8) { W(0); X(0, 0); X(0, 1); E(EV_JC); E(EV_JX); W(1); }
    }
    else if (PORD >= 6 && j == 0) { W(2); X(1, 0); X(1, 1); if (PORD != 8) E(EV_JX); E(EV_JC + 1); W(3); }
    else if (PORD >= 6 && j == 1) { X(2, 0); X(2, 1); W(4); E(EV_JC + 2); W(5); X(3, 0); }
    else if (PORD >= 6 && j == 2) { E(EV_JC + 3); W(6); X(3, 1); }
    else if (j == 0) { W(3); X(2, 0); X(2, 1); W(4); }
    else { W(j + 4); if (j % 2 == 0) X(j / 2 + 2, 1); else X((j - 1) / 2 + 3, 0); }
    if (ffn && j == 19) L[k++] = EV_P + 13;
    const int k0 = k;
    if (ffn && j > F_CHK) for (int i = (j - F_CHK - 1) * NINL / (19 - F_CHK); i < (j - F_CHK) * NINL / (19 - F_CHK); ++i) put(j, 3 + 6 * (i & 1), EV_R + 20 + i);
    if (!ffn) {
      if (j == 15) { L[k++] = EV_P + 15; }
      if (j == 16) { L[k++] = EV_P + 16; L[k++] = EV_P + 17; L[k++] = EV_P + 10; L[k++] = EV_P + 11; }
      if (j == 17) { L[k++] = EV_P + 12; L[k++] = EV_P + 13; L[k++] = EV_P + 14; L[k++] = EV_R + 20; }
      if (j == 18) for (int i = 21; i < 25; ++i) L[k++] = EV_R + i;
      if (j == 19) for (int i = 6; i < 10; ++i) L[k++] = EV_P + i;
    }
    const int last = ffn ? 23 : 14;
    for (int i = 0; i < k; ++i) put(j, j == -1 ? 0 : (PORD >= 6 && j < 2) ? (ffn ? 1 + 2 * i : 1 + i + i / 2) : j == 0 ? 2 + 2 * i : ffn ? (k > 4 && DPOS > 14 ? 14 : DPOS) + 2 * i : (j >= 15 ? 2 + 3 * i : ADPOS + i), L[i]);
    if (PORD) {} else if (JEARLY) {
      if (j == -1) { put(j, 0, EV_JC); put(j, 0, EV_JX); }
      if (j == 1) put(j, last, EV_JC + 1);
      if (j == 3) put(j, last, EV_JC + 2);
      if (j == 5) put(j, last, EV_JC + 3);
    } else {
      if (j == 1) put(j, last, EV_JC);
      if (j == 3) { put(j, last, EV_JX); put(j, last, EV_JC + 1); }
      if (j == 5) put(j, last, EV_JC + 2);
      if (j == 6) put(j, last, EV_JC + 3);
    }
    if (j == (ffn ? F_CHK : A_CHK) - 3) put(j, last, EV_FCOPY);
    if (j == J_GRAN - 3) put(j, last, EV_GCOPY);
    if (!ffn && j == (PORD >= 6 ? 1 : 0)) put(j, last, EV_TDMA);
    if (!ffn && j == A_TR + 1) put(j, 15, EV_TST);
  }
  for (int a = 1; a < n; ++a)
    for (int c = a; c > 0 && S.j[c - 1] == S.j[c] && S.m[c - 1] > S.m[c]; --c) {
      const int tm = S.m[c], tc = S.c[c]; S.m[c] = S.m[c - 1]; S.c[c] = S.c[c - 1]; S.m[c - 1] = tm; S.c[c - 1] = tc; }
  S.n = n; return S;
}
constexpr int ev_ops(int c, bool v7) {
  if (c >= EV_W) return (v7 && (c - EV_W) % 4 == 1) ? 0 : 1;
  if (c >= EV_P) return 1;
  if (c >= EV_X) return v7 ? 2 : 1;
  if (c >= EV_JC && c < EV_JX) return 1;
  return c == EV_JX ? 2 : 0;
}
constexpr int ev_due(int c, bool ffn) {
  if (c >= EV_W) return (c - EV_W) / 4 - 1;
  if (c >= EV_P) return 99;
  if (c >= EV_X) return 2 * ((c - EV_X) / 2) - 1;
  if (c >= EV_JC && c < EV_JX) return J_COLS;
  return c == EV_JX ? J_ROWS : c == EV_FCOPY ? (ffn ? F_CHK : A_CHK) : c == EV_GCOPY ? J_GRAN : c == EV_TDMA ? A_TR : c == EV_TST ? A_FLAG : 99;
}
constexpr int wait_n(const Sched& S, bool ffn, bool v7, int j) {
  int last = -1, end = 0;
  for (int i = 0; i < S.n; ++i) if (S.j[i] < j || (j == -1 && S.j[i] == -1)) { end = i + 1; if (ev_due(S.c[i], ffn) == j) last = i; }
  if (last < 0) return 63;
  int n = 0; for (int i = last + 1; i < end; ++i) n += ev_ops(S.c[i], v7);
  return n > 63 ? 63 : n;
}
constexpr Sched F_S = make_sched(true), A_S = make_sched(false);
constexpr int waitF(int j) { return wait_n(F_S, true, false, j); }
constexpr int waitA(int j) { return wait_n(A_S, false, false, j) < wait_n(A_S, false, true, j) ? wait_n(A_S, false, false, j) : wait_n(A_S, false, true, j); }
constexpr int ev_at(const Sched& S, int j, int m, int k) {
  for (int i = 0; i < S.n; ++i) if (S.j[i] == j && S.m[i] == m) { if (k == 0) return S.c[i]; --k; }
  return EV_NONE;
}

template <unsigned IMM> DI void ldev(u32x4& d, rsrc_t r, unsigned voff, unsigned soff) {
  asm volatile("buffer_load_dwordx4 %0, %1, %2, %3 offen offset:%4 sc1" : "=v"(d) : "v"(voff), "s"(r), "s"(soff), "i"(IMM) : "memory");
}
template <int F> DI void loadW(u32x4 (&Wf)[50], const P2& Q) {
  if constexpr (F % 5 < 4) ldev<(F % 5) * 1024u>(Wf[F], Q.rq, Q.lv, Q.wo[F / 5]); else ldev<3072u>(Wf[F], Q.rq, Q.lv, Q.wo[F / 5] + 1024u);
}
template <int S, int NF, int KEEP> DI void loadW_if(unsigned go, u32x4 (&Wf)[50], const P2& Q) {
  unsigned t;
  if constexpr (NF == 5)
    asm volatile("s_cmp_eq_u32 %9, 0\n\ts_cbranch_scc1 1f\n\ts_add_u32 %5, %8, 0x400\n\t"
                 "buffer_load_dwordx4 %0, %6, %7, %8 offen sc1\n\tbuffer_load_dwordx4 %1, %6, %7, %8 offen offset:1024 sc1\n\tbuffer_load_dwordx4 %2, %6, %7, %8 offen offset:2048 sc1\n\t"
                 "buffer_load_dwordx4 %3, %6, %7, %8 offen offset:3072 sc1\n\tbuffer_load_dwordx4 %4, %6, %7, %5 offen offset:3072 sc1\n\ts_waitcnt vmcnt(%10)\n1:"
                 : "+v"(Wf[5 * S]), "+v"(Wf[5 * S + 1]), "+v"(Wf[5 * S + 2]), "+v"(Wf[5 * S + 3]), "+v"(Wf[5 * S + 4]), "=&s"(t)
                 : "v"(Q.lv), "s"(Q.rq), "s"(Q.wo[S]), "s"(go), "i"(KEEP) : "memory", "scc");
  else
    asm volatile("s_cmp_eq_u32 %6, 0\n\ts_cbranch_scc1 1f\n\ts_add_u32 %2, %5, 0x400\n\t"
                 "buffer_load_dwordx4 %0, %3, %4, %5 offen offset:3072 sc1\n\tbuffer_load_dwordx4 %1, %3, %4, %2 offen offset:3072 sc1\n\ts_waitcnt vmcnt(%7)\n1:"
                 : "+v"(Wf[5 * S + 3]), "+v"(Wf[5 * S + 4]), "=&s"(t) : "v"(Q.lv), "s"(Q.rq), "s"(Q.wo[S]), "s"(go), "i"(KEEP) : "memory", "scc");
}
template <class T> DI void fresh(T& v) { asm volatile("" : "=v"(v)); }
template <int N> DI void waitv() { asm volatile("s_waitcnt vmcnt(%0)" :: "i"(N) : "memory"); }
template <int S0, int S1> DI void out_tile(const Params& p, Stamps& ST, const P2& Q, u32x4 (&Wf)[50], unsigned late, unsigned lds, int w, int lane, int mt, int n, u64 tag) {
  const rsrc_t rh = mk(p.ws + WS_H, H_BYTES);
  const int t = lane & 15, g = lane >> 4;
  const rsrc_t rx = mk(p.x, 2048u * 1280u), ro = mk(p.out, 2048u * 1280u);
  stamp(3); sstamp(34);
  const int b = blockIdx.x;
  const bool last = blockIdx.x == 0 && w >= 4;
  unsigned ov[2]; u32x4 xr[2];
  unsigned is3; asm("s_cmp_eq_u32 %1, 3\n\ts_cselect_b32 %0, 1, 0" : "=s"(is3) : "s"(w) : "scc");
  sfor<S1 - S0>([&](auto K) { loadW_if<S0 + K.value, 5, 63>(late, Wf, Q); });
  sfor<9 - S1>([&](auto K) { loadW_if<S1 + K.value, 5, 63>(w ^ 3, Wf, Q); });
  if (w == 3) poll(p.ws + WS_FH + ((mt >> 1) * 9 + (lane < 9 ? lane : 0)) * 64u, tag);
  sfor<9 - S1>([&](auto K) { loadW_if<S1 + K.value, 5, 63>(is3, Wf, Q); });
  const int jl = (mt & 15) ? (4 * (mt & 15) - 1) / 9 : 0, jh = (4 * (mt & 15) + 2) / 9;
  const unsigned fa = WS_FH + (144 + ((mt >> 4) * 7 + jl) * 8) * 64u, fn = (jh - jl + 1) * 8;
  u32x4 Hf[3][4];
  constexpr auto slot = [](int q) { return q < 7 ? q : q == 7 ? 9 : q - 1; };
  constexpr auto bi = [](int q) { return q < 6 ? (q & 1) : (q - 4) % 3; };
  auto loadH = [&](auto Qp, auto I) { ldev<I.value * 1024u>(Hf[bi(Qp.value)][I.value], rh, Q.lv, (mt * 74u + Q.st[slot(Qp.value)]) * 4096u); };
  const unsigned lq = opaque(lds + w * 1024u + Q.lv), lq2 = opaque(lq + 8 * 8192u), lq3 = opaque(lq + 16 * 8192u);
  auto row = [&](int r) { return (r < 8 ? lq : r < 16 ? lq2 : lq3) + (r & 7) * 8192u; };
  lds_bar();
  stamp(4); stamp(5); sstamp(35);
  if (BAL >= 2 && w >= 4) __builtin_amdgcn_s_setprio(1);
  sfor<4>([&](auto I) { loadH(IC<0>{}, I); }); sfor<4>([&](auto I) { loadH(IC<1>{}, I); });
  f32x4 acc[5][4];
  constexpr auto nX = [](int q) { return q == 1 ? 1 : q == 4 ? 5 : q == 5 ? 2 : 0; };
  sfor<8>([&](auto Qp) {
    constexpr int q = Qp.value, s = slot(q);
    waitv<(q >= 2 ? nX(q - 2) : 0) + 4 + (q >= 1 ? nX(q - 1) : 0)>();
    sstamp(36 + q);
    land(Hf[bi(q)][0]); land(Hf[bi(q)][1]); land(Hf[bi(q)][2]); land(Hf[bi(q)][3]);
    if constexpr (q == 4) {
      const u32x2 f = ld64(lq); const unsigned f0 = f.x, f1 = f.y, l4 = opaque(Q.lv) >> 4;
      if (!__all(l4 >= fn || (f0 == (unsigned)tag && f1 == (unsigned)(tag >> 32)))) poll(p.ws + fa + (l4 < fn ? l4 : 0) * 64u, tag);
    }
    if constexpr (s == 9) {
      sfor<4>([&](auto I) { loadH(IC<q + 2>{}, I); });
      sfor<5>([&](auto C) { land(Wf[45 + C.value]); });
      if (w < 2) sfor<5>([&](auto C) {
#pragma unroll
        for (int a = 0; a < 4; ++a) mac(acc[C.value][a], asb(Wf[45 + C.value]), asb(Hf[bi(q)][a]));
      });
    } else {
      bf16x8 wf[5];
      auto get = [&](auto C) { constexpr int f = s * 5 + C.value;
        if constexpr (f >= 20) { land(Wf[f]); wf[C.value] = asb(Wf[f]); } else wf[C.value] = ld128(row(f)); };
      get(IC<0>{}); get(IC<1>{});
      sfor<5>([&](auto C) {
        if constexpr (C.value < 3) get(IC<C.value + 2>{});
#pragma unroll
        for (int a = 0; a < 4; ++a) { if constexpr (q == 0) mac0(acc[C.value][a], wf[C.value], asb(Hf[0][a])); else mac(acc[C.value][a], wf[C.value], asb(Hf[bi(q)][a])); }
        if constexpr (q >= 4 && C.value < 4) loadH(IC<q + 2>{}, C);
      });
      if constexpr (q < 4) sfor<4>([&](auto I) { loadH(IC<q + 2>{}, I); });
    }
    if constexpr (BAL >= 2 && q == BAL) __builtin_amdgcn_s_setprio(0);
    if constexpr (BAL == 1 && (q == 1 || q == 3 || q == 5)) __builtin_amdgcn_s_barrier();
    if constexpr (q == 1) dma_dev(mk(p.ws, CTL_BYTES), (opaque(Q.lv) >> 4) < fn ? Q.lv * 4u : OOB, fa, lds + w * 1024u);
    if constexpr (q == 4) sfor<5>([&](auto K) { loadW<45 + K.value>(Wf, Q); });
    if constexpr (q == 5) {
      const unsigned l2 = opaque(Q.lv) >> 4;
#pragma unroll
      for (int i = 0; i < 2; ++i) {
        const int cc = (w + 8 * i) * 4 + (l2 >> 4), tb = (cc * 26) >> 8, c8 = cc - tb * 10;
        const int B = 4 * mt + tb, tok = 2 * ((B & 63) * 16 + (l2 & 15)) + (B >> 6);
        ov[i] = (i == 0 || w < 2) ? (unsigned)(tok * 1280 + n * 160 + c8 * 16) : OOB;
      }
      xr[0] = hload4(rx, ov[0], 0u); xr[1] = hload4(rx, ov[1], 0u);
    }
    if constexpr (q == 5) { if (last) dma_dev(mk(p.ws, CTL_BYTES), lane * 64u, WS_FH + (w - 4) * 4096u, lds + w * 1024u); }
    GAP();
  });
  waitv<3>();
  sstamp(44); sstamp(45);
  land(xr[0]); land(xr[1]);
  if (last) {
    const u32x2 f = ld64(lq); const unsigned f0 = f.x, f1 = f.y;
    if (!__all(f0 == (unsigned)tag && f1 == (unsigned)(tag >> 32))) poll(p.ws + WS_FH + ((w - 4) * 64 + lane) * 64u, tag);
  }
  sfor<10>([&](auto K) { land(Wf[35 + K.value]); });
  sfor<4>([&](auto A) {
    constexpr int a = A.value;
    if constexpr (a > 0) waitv<3 - a>();
    land(Hf[1][a]); land(Hf[2][a]);
    sfor<5>([&](auto C) { mac(acc[C.value][a], asb(Wf[35 + C.value]), asb(Hf[1][a])); GAP(); });
    sfor<5>([&](auto C) { mac(acc[C.value][a], asb(Wf[40 + C.value]), asb(Hf[2][a]));
      if constexpr (a > 0) *(LDSP f32x4*)(size_t)row(5 * (a - 1) + C.value) = acc[C.value][a - 1];
      GAP(); });
  });
  settle();
  stamp(6); sstamp(46);
#pragma unroll
  for (int c = 0; c < 5; ++c) *(LDSP f32x4*)(size_t)row(15 + c) = acc[c][3];
  lds_bar();
  if (last && w == 7 && lane == 0) put_flag(p.ws + WS_E, tag);
  stamp(7); sstamp(47);
#pragma unroll
  for (int i = 0; i < 2; ++i) {
    if (i == 0 || w < 2) {
      const int cc = (w + 8 * i) * 4 + g, tb = cc / 10, c8 = cc - tb * 10;
      const unsigned a = lds + (unsigned)(tb * 5 + (c8 >> 1)) * 8192u + (c8 & 1) * 512u + t * 16u;
      f32x4 lo[8], hi[8];
#pragma unroll
      for (int k = 0; k < 8; ++k) { lo[k] = ld128f(a + k * 1024u); hi[k] = ld128f(a + k * 1024u + 256u); }
      const f32x4 l = ((lo[0] + lo[1]) + (lo[2] + lo[3])) + ((lo[4] + lo[5]) + (lo[6] + lo[7])), hh = ((hi[0] + hi[1]) + (hi[2] + hi[3])) + ((hi[4] + hi[5]) + (hi[6] + hi[7]));
      const unsigned x0 = xr[i].x, x1 = xr[i].y, x2 = xr[i].z, x3 = xr[i].w;
      const u32x4 o = {pk(l.x + blo(x0), l.y + bhi(x0)), pk(l.z + blo(x1), l.w + bhi(x1)), pk(hh.x + blo(x2), hh.y + bhi(x2)), pk(hh.z + blo(x3), hh.w + bhi(x3))};
      __builtin_amdgcn_raw_buffer_store_b128(o, ro, ov[i], 0, 0);
    }
  }
  sstamp(48);
  dump_sv();
}

constexpr unsigned F_WS = 24576, F_XR = 98304, F_XS = 16384, F_EX = 147456;
constexpr unsigned FX_G = F_EX, FX_CS = F_EX + 11264, FX_ST = FX_CS + 1536, FX_FW = FX_ST + 1024, FX_JP = F_EX;
DI void ffn_tile(const Params& p, Stamps& ST, unsigned lds, int b, int w, int lane, int par, int rtl, int ct, int mt, int n2, int myflag, u64& tag) {
  P2 Q;
  const int mg = w >> 2, ng = w & 3;
  const int t = lane & 15, g = lane >> 4;
  const rsrc_t rx = mk(p.x, 2048u * 1280u), r1 = mk(p.w1, 640u * 3456u), r3 = mk(p.w3, 640u * 3456u);
  const rsrc_t rm = mk(sel(mg, p.w3, p.w1), 640u * 3456u), rctl = mk(p.ws, CTL_BYTES);
  const unsigned wv = (unsigned)(8 * ng + (lane >> 3)) * 3456u + (unsigned)((((lane & 7) >> 1) ^ (((lane >> 4) & 1) | ((ng & 1) << 1))) * 32 + (lane & 1) * 16);
  const unsigned ws0 = ct * 384u + mg * 128u, ws1 = ct * 384u + (mg ? 0u : 256u);
  const unsigned xv = (unsigned)(2 * (8 * w + (lane >> 3))) * 1280u + (unsigned)(((lane & 7) ^ (((lane >> 4) & 3) | ((w & 1) << 2))) * 16);
  const unsigned xs0 = (unsigned)(2 * 128 * rtl + par) * 1280u;
  const unsigned lw = lds + w * 1024u;
  Job J; u32x4 Wf[50];
  auto event = [&](auto C) {
    constexpr int c = C.value;
    if constexpr (c >= EV_W) {
      constexpr int s = (c - EV_W) / 4, i = (c - EV_W) % 4;
      dmai<s * 32u * 3456u + (i == 2 ? 128u : 0u), ((s & 3) * 3 + i) * 8192u>(i == 0 ? r1 : i == 1 ? rm : r3, wv, i == 1 ? ws1 : ws0, lw);
    } else if constexpr (c >= EV_R) loadW<c - EV_R>(Wf, Q);
    else if constexpr (c >= EV_P) dmai_dev<((c - EV_P) % 5) * 1024u, (c - EV_P) * 8192u>(Q.rq, Q.lv, Q.wo[(c - EV_P) / 5], lw);
    else if constexpr (c >= EV_X) {
      constexpr int s = (c - EV_X) / 2, h = (c - EV_X) % 2;
      dmai<s * 128u + h * (128u * 1280u), (12 + (s % 3) * 2 + h) * 8192u>(rx, xv, xs0, lw);
    } else if constexpr (c >= EV_JC && c < EV_JX) { if constexpr (c == EV_JC) job_setup(J, p, b, w, lane); job_loadc<c - EV_JC>(J); }
    else if constexpr (c == EV_JX) job_loadx(J, rx, b, w, lane);
    else if constexpr (c == EV_FCOPY) { if (w < 2) copy_wflags(rctl, lds + FX_FW, w, lane); }
    else if constexpr (c == EV_GCOPY) {
      if (w < 6) {
        const unsigned so = WS_GC + (unsigned)((w % 3) * 5376 + 1920 + (w / 3) * 1728 + 192 * ct) * 8u;
        dma_dev(rctl, Q.lv, so, lds + FX_G + w * 1536u);
        if (lane < 32) dma_dev(rctl, Q.lv, so + 1024u, lds + FX_G + w * 1536u + 1024u);
      } else dma_dev(rctl, Q.lv, WS_GR + (unsigned)(par * 1024 + rtl * 128 + (w - 6) * 64) * 16u, lds + FX_G + 9216u + (w - 6) * 1024u);
    }
  };
  sstamp(31);
  sfor<18>([&](auto K) { event(IC<ev_at(F_S, -1, 0, K.value)>{}); GAP(); });
  sstamp(32);
  Q.rq = mk(p.ws + WS_W, W_BYTES); Q.lv = lane * 16u;
  unsigned xa[2], wa[2][3];
  xa[0] = lds + F_XR + (unsigned)(64 * mg + t) * 128u + (unsigned)((g ^ (t >> 1)) * 16); xa[1] = opaque(xa[0] ^ 64u);
  {
    const int q = t >> 2, pp = t & 3, f = (q >> 1) | ((g & 1) << 1);
#pragma unroll
    for (int i = 0; i < 3; ++i) { const int fb = 3 * ng + i; wa[0][i] = lds + (fb >> 2) * 4096u + (8 * g + q) * 128u + (((fb & 3) ^ f) * 32) + 8 * pp; wa[1][i] = opaque(wa[0][i] + 2 * F_WS); }
  }
  f32x4 acc[6][4];
  bf16x8 X[2][4]; u32x2 W[2][12];
  auto rd = [&](auto S, auto R) {
    constexpr int s = S.value, r = R.value;
    if constexpr (r < 4) X[s & 1][r] = ld128(xa[s & 1] + ((s >> 1) % 3) * F_XS + r * 2048u);
    else { constexpr int k = r - 4, bb = k >> 1; W[s & 1][k] = tr64(wa[(s >> 1) & 1][bb % 3] + (s & 1) * F_WS + (bb / 3) * 12288u + (k & 1) * 512u); }
  };
  tag = first_bar<waitF(-1)>(p.ws) + 1;
  stamp(1); cstamp(c0); sstamp(33);
  sfor<16>([&](auto R) { rd(IC<0>{}, R); });
  sfor<20>([&](auto JC) {
    constexpr int j = JC.value;
    wait_bar<waitF(j)>();
    sstamp(j);
    constexpr int lead = (j == J_ROWS + 1 || j == J_COLS + 1 || j == J_GRAN + 1 || j == F_CHK) ? 1 : 0;
    if constexpr (PRIO && lead) { if (mg) __builtin_amdgcn_s_setprio(1); }
    if constexpr (j == J_ROWS) { land(J.xa); land(J.xb); }
    if constexpr (j == J_COLS) { land(J.c[0]); land(J.c[1]); land(J.c[2]); land(J.c[3]); }
    if constexpr (j == F_CHK) { int w2 = w; asm volatile("" : "+s"(w2)); p2_setup(Q, n2, w2); }
    GAP();
    sfor<24>([&](auto MC) {
      constexpr int m = MC.value, bb = m / 4, a = m % 4;
      if constexpr (j == 0) mac0(acc[bb][a], cat(W[0][2 * bb], W[0][2 * bb + 1]), X[0][a]); else mac(acc[bb][a], cat(W[j & 1][2 * bb], W[j & 1][2 * bb + 1]), X[j & 1][a]);
      if constexpr (j < 19 && m >= RPOS && m < RPOS + 16) rd(IC<j + 1>{}, IC<m - RPOS>{});
      sfor<3>([&](auto K) { event(IC<ev_at(F_S, j, m, K.value)>{}); });
      GAP();
    });
    if constexpr (j == J_ROWS || j == J_ROWS + 1) { if (mg == j - J_ROWS) job_rows(J, rctl, b, w, lane, (unsigned)tag); }
    if constexpr (j == J_COLS || j == J_COLS + 1) { if (mg == j - J_COLS) job_cols(J, lds + FX_JP, w, lane); }
    if constexpr (j == J_PUB) { if (w == 1) job_pubc(lds + FX_JP, rctl, b, lane, (unsigned)tag); }
    if constexpr (j == F_CHK) { if (w == 7) check_wflags(p, lds + FX_FW, lane, tag); }
    if constexpr (j == J_GRAN || j == J_GRAN + 1) {
      if (mg == j - J_GRAN) {
        const int tid = w * 64 + lane;
        if (w < 6) {
          const int up = w >= 3, c = tid - 192 * up;
          *(LDSP float*)(size_t)(lds + FX_CS + tid * 4u) = colsum(lds + FX_G + up * 4608u + c * 8u, 1536u, p.ws, 1920 + up * 1728 + 192 * ct + c, (unsigned)tag);
        } else *(LDSP f32x2*)(size_t)(lds + FX_ST + (tid - 384) * 8u) = rowstat(lds + FX_G + 9216u + (tid - 384) * 16u, p.ws, par * 1024 + rtl * 128 + tid - 384, (unsigned)tag);
      }
    }
    if constexpr (PRIO && lead) __builtin_amdgcn_s_setprio(0);
    GAP();
  });
  settle();
  stamp(2); cstamp(c1); sstamp(20);
  f32x4 cG[3], cU[3]; f32x2 stt[4];
#pragma unroll
  for (int i = 0; i < 3; ++i) { cG[i] = ld128f(lds + FX_CS + ((3 * ng + i) * 16 + 4 * g) * 4u); cU[i] = ld128f(lds + FX_CS + 768u + ((3 * ng + i) * 16 + 4 * g) * 4u); }
#pragma unroll
  for (int a = 0; a < 4; ++a) stt[a] = ld64f(lds + FX_ST + (64 * mg + 16 * a + t) * 8u);
  const rsrc_t rh = mk(p.ws + WS_H, H_BYTES);
  const unsigned hv = (unsigned)(lane >> 5) * 1024u + (lane & 31) * 16u;
  const unsigned hs = (unsigned)(2 * (8 * par + rtl) + mg) * (74u * 4096u);
  u32x4 HO[2][3];
  sfor<2>([&](auto AP) {
    constexpr int ap = AP.value;
    u32x2 P[2][3];
    sfor<2>([&](auto A2) {
      constexpr int a2 = A2.value, a = 2 * ap + a2;
      const f32x2 st = stt[a];
      const f32x2 nm = bc2(-st.x), k1 = bc2(st.y * -1.44269504089f), ik = bc2(__builtin_amdgcn_rcpf(st.y * st.y));
      f32x2 G[6], U[6], E[6];
#pragma unroll
      for (int i = 0; i < 3; ++i) { G[2 * i] = fma2(nm, lo2(cG[i]), lo2(acc[i][a])); G[2 * i + 1] = fma2(nm, hi2(cG[i]), hi2(acc[i][a])); }  GAP();
#pragma unroll
      for (int k = 0; k < 6; ++k) E[k] = G[k] * k1;
#pragma unroll
      for (int i = 0; i < 3; ++i) { U[2 * i] = fma2(nm, lo2(cU[i]), lo2(acc[3 + i][a])); U[2 * i + 1] = fma2(nm, hi2(cU[i]), hi2(acc[3 + i][a])); }
      GAP();
#pragma unroll
      for (int k = 0; k < 6; ++k) E[k] = (f32x2){__builtin_amdgcn_exp2f(E[k].x), __builtin_amdgcn_exp2f(E[k].y)};  GAP();
#pragma unroll
      for (int k = 0; k < 6; ++k) { E[k] = fma2(E[k], ik, ik); G[k] = G[k] * U[k]; }  GAP();
#pragma unroll
      for (int k = 0; k < 6; ++k) E[k] = (f32x2){__builtin_amdgcn_rcpf(E[k].x), __builtin_amdgcn_rcpf(E[k].y)};  GAP();
#pragma unroll
      for (int k = 0; k < 6; ++k) G[k] = G[k] * E[k];  GAP();
#pragma unroll
      for (int i = 0; i < 3; ++i) P[a2][i] = (u32x2){pk2(G[2 * i]), pk2(G[2 * i + 1])};
      GAP();
    });
#pragma unroll
    for (int i = 0; i < 3; ++i) { HO[ap][i] = halffrag(P[0][i], P[1][i]); pin(HO[ap][i]); }
    if constexpr (MIDST && ap == 0) {
#pragma unroll
      for (int k = 0; k < 3; ++k) { const int fb = 12 * ct + 3 * ng + k; st128_dev(HO[0][k], rh, hv, hs + (20 + (fb >> 1)) * 4096u + (fb & 1) * 512u); }
    }
  });
#pragma unroll
  for (int k = MIDST ? 3 : 0; k < 6; ++k) {
    const int fb = 12 * ct + 3 * ng + k % 3;
    st128_dev(HO[k / 3][k % 3], rh, hv, hs + (20 + (fb >> 1)) * 4096u + (k / 3) * 2048u + (fb & 1) * 512u);
  }
  sfor<25 - NINL>([&](auto K) { fresh(Wf[20 + NINL + K.value]); });
  sfor<NOLD>([&](auto K) { loadW_if<4 + NINL / 5 + K.value, 5, KEEPW>(1 - mg, Wf, Q); });
  sstamp(21);
  if (mg) wait_bar<0>(); else wait_bar<5 * NOLD>();
  sstamp(22); sstamp(23); sstamp(24); sstamp(25); sstamp(26); sstamp(27); sstamp(28);
  if (w == 0 && lane == 0) put_flag(p.ws + WS_FH + myflag * 64u, tag);
  dmai_dev<3072u, 18 * 8192u>(Q.rq, Q.lv, Q.wo[3], lw); dmai_dev<4096u, 19 * 8192u>(Q.rq, Q.lv, Q.wo[3], lw);
  out_tile<4 + NINL / 5, 4 + NINL / 5 + NOLD>(p, ST, Q, Wf, mg, lds, w, lane, mt, n2, tag);
}

constexpr unsigned A_WS = 15360, A_XR = 61440, A_XS = 20480, A_JOB = 122880;
constexpr unsigned AX_G = 147456, AX_JP = AX_G, AX_FW = AX_G + 9216, AX_CS = AX_FW + 2048, AX_ST = AX_CS + 1024, AX_Z = AX_ST + 1536;
DI void att_tile(const Params& p, Stamps& ST, unsigned lds, int b, int w, int lane, int par, int jt, int hg, int job, int mt, int n2, int myflag, u64& tag) {
  P2 Q;
  const int mg = w >> 2, h = (w + mg) & 3;
  const int t = lane & 15, g = lane >> 4;
  const rsrc_t rx = mk(p.x, 2048u * 1280u), rw = mk(p.wqkv, 640u * 3840u), rctl = mk(p.ws, CTL_BYTES);
  unsigned wv[2];
#pragma unroll
  for (int i = 0; i < 2; ++i) {
    const unsigned n = (2 * w + i) * 64 + lane, pos = (n * 2185u) >> 16, cc = n - pos * 30, sec = (cc * 26u) >> 8;
    const unsigned k = (pos & 19) | ((pos & 4) << 1) | ((pos & 8) >> 1);
    wv[i] = k * 3840u + sec * 1120u + cc * 16u;
  }
  const unsigned ws0 = hg * 160u;
  const unsigned xv = (unsigned)(2 * (8 * w + (lane >> 3))) * 1280u + (unsigned)(((lane & 7) ^ (((lane >> 4) & 3) | ((w & 1) << 2))) * 16);
  const unsigned xs0 = (unsigned)(2 * 144 * jt + par) * 1280u;
  const bool isO = job < 20;
  const unsigned lw = lds + w * 1024u, lw2 = lds + w * 2048u;
  Job J; u32x4 Wf[50];
  auto event = [&](auto C) {
    constexpr int c = C.value;
    if constexpr (c >= EV_W) {
      constexpr int s = (c - EV_W) / 4, i = (c - EV_W) % 4;
      if (i == 0 || w < 7) dmai<s * (32u * 3840u), (s & 3) * A_WS + i * 1024u>(rw, wv[i], ws0, lw2);
    } else if constexpr (c >= EV_R) loadW<c - EV_R>(Wf, Q);
    else if constexpr (c >= EV_P) dmai_dev<((c - EV_P) % 5) * 1024u, (c - EV_P) * 8192u>(Q.rq, Q.lv, Q.wo[(c - EV_P) / 5], lw);
    else if constexpr (c == EV_FCOPY) { if (w < 2) copy_wflags(rctl, lds + AX_FW, w, lane); }
    else if constexpr (c >= EV_X) {
      constexpr int s = (c - EV_X) / 2, hf = (c - EV_X) % 2;
      constexpr unsigned so = s * 128u + hf * (160u * 1280u), l = A_XR + (s % 3) * A_XS + hf * 10240u;
      dmai<so, l>(rx, xv, xs0, lw);
      if (w >= 6) dmai<so + 32u * 1280u, l + 2048u>(rx, xv, xs0, lw);
    } else if constexpr (c >= EV_JC && c < EV_JX) { if constexpr (c == EV_JC) job_setup(J, p, b, w, lane); job_loadc<c - EV_JC>(J); }
    else if constexpr (c == EV_JX) job_loadx(J, rx, b, w, lane);
    else if constexpr (c == EV_TDMA) {
      if (job < 74) {
        const rsrc_t rj = mk(sel(isO, p.wo, p.w2), isO ? 640u * 1280u : 1728u * 1280u);
        const int db = 2 * job + (w >> 2), q5 = db / 5, e = db - 5 * q5;
        const int u = !isO ? 32 * (job - 20) + 4 * w : e < 4 ? 20 * (4 * q5 + e) + 4 * (w & 3) : 20 * (4 * q5 + (w & 3)) + 16;
        const unsigned v = g * 1280u + (t ^ (2 * (g | ((w & 1) << 2)))) * 16u;
#pragma unroll
        for (int i = 0; i < 5; ++i) dma(rj, v, u * 1280u + i * 256u, lds + A_JOB + (5 * w + i) * 1024u);
      }
    } else if constexpr (c == EV_GCOPY) {
      if (lane < 40) {
        dma_dev(rctl, Q.lv, WS_GC + (unsigned)((w % 3) * 5376 + (w / 3) * 640 + 80 * hg) * 8u, lds + AX_G + w * 640u);
        if (w == 0) dma_dev(rctl, Q.lv, WS_GC + (unsigned)(2 * 5376 + 2 * 640 + 80 * hg) * 8u, lds + AX_G + 8 * 640u);
      }
      if (w >= 1 && w < 4) dma_dev(rctl, (w < 3 || lane < 32) ? Q.lv : OOB, WS_GR + (unsigned)(par * 1024 + jt * 144 + (w - 1) * 64) * 16u, lds + AX_G + 6144u + (w - 1) * 1024u);
    }
  };
  sstamp(31);
  sfor<15>([&](auto K) { event(IC<ev_at(A_S, -1, 0, K.value)>{}); GAP(); });
  sstamp(32);
  Q.rq = mk(p.ws + WS_W, W_BYTES); Q.lv = lane * 16u;
  unsigned xa[2], wm, wt;
  xa[0] = lds + A_XR + (unsigned)(80 * mg + t) * 128u + (unsigned)((g ^ (t >> 1)) * 16); xa[1] = opaque(xa[0] ^ 64u);
  {
    const int q = t >> 2, pp = t & 3;
    const unsigned rowb = lds + (unsigned)(q + 4 * (g & 1) + 16 * (g >> 1)) * 480u;
    wm = rowb + 40 * h + 8 * pp; wt = rowb + 40 * pp + 32 + h * 160;
  }
  f32x4 acc[4][5];
#pragma unroll
  for (int c = 0; c < 5; ++c) { acc[3][c] = (f32x4){0.f, 0.f, 0.f, 0.f}; pin(acc[3][c]); }
  bf16x8 X[2][5]; u32x2 W[2][8];
  auto rd = [&](auto S, auto R) {
    constexpr int s = S.value, r = R.value;
    if constexpr (r < 5) X[s & 1][r] = ld128(xa[s & 1] + ((s >> 1) % 3) * A_XS + r * 2048u);
    else { constexpr int k = r - 5; W[s & 1][k] = tr64((k < 6 ? wm + (k >> 1) * 160u : wt) + (s & 3) * A_WS + (k & 1) * 3840u); }
  };
  W[0][6] = W[0][7] = W[1][6] = W[1][7] = (u32x2){0u, 0u};
  bool vp[4]; float bias[4], bias0[4];
#pragma unroll
  for (int r = 0; r < 4; ++r) { const int kk = 4 * g + r; vp[r] = kk >= t + 9; const bool vc = kk <= t && kk >= t - 7;
    bias[r] = (vp[r] || vc) ? 0.f : -INFINITY; bias0[r] = ((vp[r] && mg) || vc) ? 0.f : -INFINITY; pin(bias[r]); pin(bias0[r]); }
  unsigned mp0 = (vp[0] ? 0xffffu : 0u) | (vp[1] ? 0xffff0000u : 0u), mp1 = (vp[2] ? 0xffffu : 0u) | (vp[3] ? 0xffff0000u : 0u);
  int dq = 4 * g - t - 9;
  pin(mp0); pin(mp1); pin(dq);
  const int B0 = par * 64 + 9 * jt;
  unsigned ov[4];
#pragma unroll
  for (int ap = 0; ap < 4; ++ap) {
    const int lb = (ap < 3 ? 5 * mg + 2 * ap : 2 * w) + (lane >> 5), B = B0 + lb;
    const bool ok = (lb > 0 || jt == 0) && (ap < 2 || (ap == 2 ? lane < 32 : w < 5));
    ov[ap] = ok ? (unsigned)(B >> 2) * (74u * 4096u) + (B & 3) * 1024u + (lane & 31) * 16u : OOB; pin(ov[ap]);
  }
  unsigned qz = g == h ? lds + 2048u + (unsigned)(5 * mg) * 4096u + t * 32u + g * 8u + 4 * 512u : lds + AX_Z, qs = g == h ? 4096u : 0u;
  pin(qz); pin(qs);
  tag = first_bar<waitA(-1)>(p.ws) + 1;
  stamp(1); cstamp(c0); sstamp(33);
  sfor<11>([&](auto R) { rd(IC<0>{}, R); });
  if (h < 3) { rd(IC<0>{}, IC<11>{}); rd(IC<0>{}, IC<12>{}); }
  sfor<20>([&](auto JC) {
    constexpr int j = JC.value;
    wait_bar<waitA(j)>();
    sstamp(j);
    if constexpr (j == A_FLAG) { if (job < 74 && w == 0 && lane == 0) put_flag(p.ws + WS_FW + job * 128u, tag); }
    constexpr int lead = (j == A_ROWS + 1 || j == A_COLS + 1 || j == J_GRAN + 1 || j == A_TR + 1) ? 1 : 0;
    if constexpr (PRIO && lead) { if (mg) __builtin_amdgcn_s_setprio(1); }
    if constexpr (j == J_ROWS) { land(J.xa); land(J.xb); }
    if constexpr (j == J_COLS) { land(J.c[0]); land(J.c[1]); land(J.c[2]); land(J.c[3]); }
    if constexpr (j == A_CHK) { int w2 = w; asm volatile("" : "+s"(w2)); p2_setup(Q, n2, w2); }
    GAP();
    if (h < 3) {
      if constexpr (j < 19) { rd(IC<j + 1>{}, IC<11>{}); rd(IC<j + 1>{}, IC<12>{}); }
#pragma unroll
      for (int a = 0; a < 5; ++a) mac(acc[3][a], cat(W[j & 1][6], W[j & 1][7]), X[j & 1][a]);
    }
    GAP();
    sfor<15>([&](auto MC) {
      constexpr int m = MC.value, bb = m / 5, a = m % 5;
      if constexpr (j == 0) mac0(acc[bb][a], cat(W[0][2 * bb], W[0][2 * bb + 1]), X[0][a]); else mac(acc[bb][a], cat(W[j & 1][2 * bb], W[j & 1][2 * bb + 1]), X[j & 1][a]);
      if constexpr (j < 19 && m >= ARPOS && m < ARPOS + 11) rd(IC<j + 1>{}, IC<m - ARPOS>{});
      sfor<3>([&](auto K) { event(IC<ev_at(A_S, j, m, K.value)>{}); });
      GAP();
    });
    if constexpr (j == A_TR || j == A_TR + 1) {
      if (mg == j - A_TR && job < 74) {
        const unsigned a = lds + A_JOB + (unsigned)(4 * (g >> 1) + (g & 1)) * 5120u + (t >> 2) * 256u + (t & 3) * 8u, sw = ((t >> 2) | ((g & 1) << 2)) << 5;
        u32x2 TR[10];
#pragma unroll
        for (int m = 0; m < 10; ++m) { const unsigned cb = 160u * w + 32u * (m >> 1); TR[m] = tr64(a + (m & 1) * 10240u + (cb >> 8) * 1024u + ((cb & 255u) ^ sw)); }
#pragma unroll
        for (int cb = 0; cb < 5; ++cb)
          st128_dev((u32x4){TR[2 * cb].x, TR[2 * cb].y, TR[2 * cb + 1].x, TR[2 * cb + 1].y}, Q.rq, Q.lv, (unsigned)(w * 74 + job) * 5120u + cb * 1024u);
      }
    }
    if constexpr (j == A_ROWS || j == A_ROWS + 1) { if (mg == j - A_ROWS) job_rows(J, rctl, b, w, lane, (unsigned)tag); }
    if constexpr (j == A_COLS || j == A_COLS + 1) { if (mg == j - A_COLS) job_cols(J, lds + AX_JP, w, lane); }
    if constexpr (j == A_COLS + 2) { if (w == 1) job_pubc(lds + AX_JP, rctl, b, lane, (unsigned)tag); }
    if constexpr (j == A_CHK) { if (w == 3) check_wflags(p, lds + AX_FW, lane, tag); }
    if constexpr (j == J_GRAN) {
      if (w < 4) {
        const int tid = w * 64 + lane, c = tid < 240 ? tid : 0, sec = c / 80, cc = c - 80 * sec;
        *(LDSP float*)(size_t)(lds + AX_CS + tid * 4u) = colsum(lds + AX_G + sec * 1920u + cc * 8u, 640u, p.ws, sec * 640 + 80 * hg + cc, (unsigned)tag);
      }
    }
    if constexpr (j == J_GRAN + 1) {
      if (w == 7) *(LDSP u32x2*)(size_t)(lds + AX_Z) = (u32x2){0u, 0u};
      else if (w >= 4) {
        const int r0 = (w - 4) * 64 + lane, r = r0 < 160 ? r0 : 0;
        *(LDSP f32x2*)(size_t)(lds + AX_ST + r0 * 8u) = rowstat(lds + AX_G + 6144u + r * 16u, p.ws, par * 1024 + jt * 144 + r, (unsigned)tag);
      }
    }
    if constexpr (PRIO && lead) __builtin_amdgcn_s_setprio(0);
    GAP();
  });
  settle();
  stamp(2); cstamp(c1); sstamp(20);
  constexpr unsigned E_KB = 0, E_X = 2048, E_OT = E_X + 40960;
  auto pre = [&](auto K) { loadW<25 + K.value>(Wf, Q); };
  u32x4 QT[5], KT[6];
  {
    f32x4 c4[4]; f32x2 stt[5];
#pragma unroll
    for (int bb = 0; bb < 3; ++bb) c4[bb] = ld128f(lds + AX_CS + (bb * 80 + 20 * h + 4 * g) * 4u);
    c4[3] = ld128f(lds + AX_CS + ((h < 3 ? h : 0) * 80 + 20 * g + 16) * 4u);
#pragma unroll
    for (int a = 0; a < 5; ++a) stt[a] = ld64f(lds + AX_ST + (80 * mg + 16 * a + t) * 8u);
    const unsigned xw = lds + E_X + (unsigned)(5 * mg) * 4096u + h * 512u + t * 32u + g * 8u;
    sfor<5>([&](auto A) {
      constexpr int a = A.value;
      const f32x2 nm = bc2(-stt[a].x), rs = bc2(stt[a].y);
      f32x2 v[8];
#pragma unroll
      for (int bb = 0; bb < 4; ++bb) { v[2 * bb] = fma2(nm, lo2(c4[bb]), lo2(acc[bb][a])); v[2 * bb + 1] = fma2(nm, hi2(c4[bb]), hi2(acc[bb][a])); }
      pre(IC<2 * a>{}); GAP();
#pragma unroll
      for (int k = 0; k < 8; ++k) v[k] = v[k] * rs;
      pre(IC<2 * a + 1>{}); GAP();
      QT[a].x = pk2(v[0]); QT[a].y = pk2(v[1]); KT[a + 1].x = pk2(v[2]); KT[a + 1].y = pk2(v[3]);
      *(LDSP u32x2*)(size_t)(xw + a * 4096u) = (u32x2){pk2(v[4]), pk2(v[5])};
      *(LDSP u32x2*)(size_t)(xw + a * 4096u + 2048u) = (u32x2){pk2(v[6]), pk2(v[7])};
      { unsigned q0 = QT[a].x, q1 = QT[a].y, k0 = KT[a + 1].x, k1 = KT[a + 1].y; pin(q0); pin(q1); pin(k0); pin(k1); QT[a].x = q0; QT[a].y = q1; KT[a + 1].x = k0; KT[a + 1].y = k1; }
      GAP();
    });
    *(LDSP u32x2*)(size_t)(lds + E_KB + (mg ? 4096u + 43008u : 0u) + h * 512u + lane * 8u) = (u32x2){KT[5].x, KT[5].y};
  }
  sstamp(21);
  lds_bar();
  sstamp(22);
  const rsrc_t rh = mk(p.ws + WS_H, H_BYTES);
  {
    const unsigned xk = opaque(lds + E_X + (unsigned)(5 * mg) * 4096u + t * 32u + g * 8u + 5 * 512u);
    const unsigned xt = opaque(lds + E_X + (unsigned)(5 * mg) * 4096u + (unsigned)(4 * g + (t >> 2)) * 32u + (t & 3) * 8u + 6 * 512u);
    const unsigned xv2 = opaque(xt + h * 512u - 6 * 512u);
    const unsigned xv3 = opaque(xv2), xt3 = opaque(xt);
    const unsigned pb = mg ? 4096u : 0u;
    u32x4 VV[5], TT[5];
    { const u32x2 k0 = ld64(lds + E_KB + h * 512u + lane * 8u), k1 = ld64(xk - pb); KT[0] = (u32x4){k0.x, k0.y, k1.x, k1.y}; }
#pragma unroll
    for (int a = 0; a < 5; ++a) {
      const u32x2 q = ld64v(qz + a * qs), k = ld64v(xk + a * 4096u);
      QT[a].z = q.x; QT[a].w = q.y; KT[a + 1].z = k.x; KT[a + 1].w = k.y;
    }
#pragma unroll
    for (int a = 0; a < 5; ++a) {
      const u32x2 v0 = tr64(a ? xv2 + (a - 1) * 4096u : xv2 - pb), v1 = tr64(xv3 + a * 4096u), t0 = tr64(a ? xt + (a - 1) * 4096u : xt - pb), t1 = tr64(xt3 + a * 4096u);
      VV[a] = (u32x4){v0.x, v0.y, v1.x, v1.y}; TT[a] = (u32x4){t0.x, t0.y, t1.x, t1.y};
    }  GAP();
    const float c = 0.22360679775f * 1.44269504089f;
    const f32x4 z = {0.f, 0.f, 0.f, 0.f};
    f32x4 sp[5], sc[5];
#pragma unroll
    for (int a = 0; a < 5; ++a) { sp[a] = mfma(asb(KT[a]), asb(QT[a]), z); sc[a] = mfma(asb(KT[a + 1]), asb(QT[a]), z); }  pre(IC<10>{}); GAP();
    float s[5][4], mx[5], my[5];
    pin(dq);
#pragma unroll
    for (int r = 0; r < 4; ++r) vp[r] = dq >= -r;
#pragma unroll
    for (int a = 0; a < 5; ++a)
#pragma unroll
      for (int r = 0; r < 4; ++r) s[a][r] = vp[r] ? sp[a][r] : sc[a][r];  pre(IC<11>{}); GAP();
#pragma unroll
    for (int a = 0; a < 5; ++a)
#pragma unroll
      for (int r = 0; r < 4; ++r) s[a][r] = fmaf(s[a][r], c, a == 0 ? bias0[r] : bias[r]);  pre(IC<12>{}); GAP();
#pragma unroll
    for (int a = 0; a < 5; ++a) mx[a] = max3(s[a][0], s[a][1], s[a][2]);
    GAP();
#pragma unroll
    for (int a = 0; a < 5; ++a) mx[a] = max2(mx[a], s[a][3]);
    pre(IC<13>{}); GAP();
#pragma unroll
    for (int a = 0; a < 5; ++a) { auto r = __builtin_amdgcn_permlane16_swap(asu(mx[a]), asu(mx[a]), false, false); unsigned r0 = r[0], r1 = r[1]; mx[a] = asf(r0); my[a] = asf(r1); }  GAP();
#pragma unroll
    for (int a = 0; a < 5; ++a) mx[a] = max2(mx[a], my[a]);
    pre(IC<14>{}); GAP();
#pragma unroll
    for (int a = 0; a < 5; ++a) { auto r = __builtin_amdgcn_permlane32_swap(asu(mx[a]), asu(mx[a]), false, false); unsigned r0 = r[0], r1 = r[1]; mx[a] = asf(r0); my[a] = asf(r1); }
    GAP();
#pragma unroll
    for (int a = 0; a < 5; ++a) mx[a] = max2(mx[a], my[a]);  pre(IC<15>{}); GAP();
#pragma unroll
    for (int a = 0; a < 5; ++a)
#pragma unroll
      for (int r = 0; r < 4; ++r) s[a][r] -= mx[a];  pre(IC<16>{}); GAP();
#pragma unroll
    for (int a = 0; a < 5; ++a)
#pragma unroll
      for (int r = 0; r < 4; ++r) s[a][r] = __builtin_amdgcn_exp2f(s[a][r]);   pre(IC<17>{}); GAP();
    unsigned p0[5], p1[5];
#pragma unroll
    for (int a = 0; a < 5; ++a) { p0[a] = pk(s[a][0], s[a][1]); p1[a] = pk(s[a][2], s[a][3]); }  pre(IC<18>{}); GAP();
    f32x4 om[5], ot[5], ol[5];
    const bf16x8 ones = asb((u32x4){0x3f803f80u, 0x3f803f80u, 0x3f803f80u, 0x3f803f80u});
#pragma unroll
    for (int a = 0; a < 5; ++a) {
      const bf16x8 pf = asb((u32x4){p0[a] & mp0, p1[a] & mp1, p0[a] & ~mp0, p1[a] & ~mp1});
      ol[a] = mfma(ones, pf, z); om[a] = mfma(asb(VV[a]), pf, z); ot[a] = mfma(asb(TT[a]), pf, z);
    }  pre(IC<19>{}); GAP();
    float il[5];
#pragma unroll
    for (int a = 0; a < 5; ++a) il[a] = __builtin_amdgcn_rcpf(ol[a].x);
    GAP();
    f32x2 o2[5][4];
#pragma unroll
    for (int a = 0; a < 5; ++a) { const f32x2 i2 = bc2(il[a]); o2[a][0] = lo2(om[a]) * i2; o2[a][1] = hi2(om[a]) * i2; o2[a][2] = lo2(ot[a]) * i2; o2[a][3] = hi2(ot[a]) * i2; }  GAP();
    u32x2 O[6], Tl[5];
#pragma unroll
    for (int a = 0; a < 5; ++a) { O[a] = (u32x2){pk2(o2[a][0]), pk2(o2[a][1])}; Tl[a] = (u32x2){pk2(o2[a][2]), pk2(o2[a][3])}; }
    GAP();
    if (g == h) {
#pragma unroll
      for (int a = 0; a < 5; ++a) *(LDSP u32x2*)(size_t)(lds + E_OT + (5 * mg + a) * 512u + ((h & 1) * 16 + t) * 16u + (h >> 1) * 8u) = Tl[a];
    }
    O[5] = O[4];
    const int db = 5 * hg + h;
    const unsigned so = (db >> 1) * 4096u + (db & 1) * 512u;
#pragma unroll
    for (int ap = 0; ap < 3; ++ap) st128_dev(halffrag(O[2 * ap], O[2 * ap + 1]), rh, ov[ap], so);
  }
  sstamp(23);
  lds_bar();
  sstamp(24);
  {
    const int db = 5 * hg + 4;
    st128_dev(ld128u(lds + E_OT + (w < 5 ? w : 0) * 1024u + lane * 16u), rh, ov[3], (db >> 1) * 4096u + (db & 1) * 512u);
  }
  sstamp(25);
  wait_bar<0>();
  sstamp(26); sstamp(27); sstamp(28);
  if (w == 0 && lane == 0) put_flag(p.ws + WS_FH + myflag * 64u, tag);
  sfor<6>([&](auto R) { dmai_dev<(R.value % 5) * 1024u, R.value * 8192u>(Q.rq, Q.lv, Q.wo[R.value / 5], lw); });
  dmai_dev<3072u, 18 * 8192u>(Q.rq, Q.lv, Q.wo[3], lw); dmai_dev<4096u, 19 * 8192u>(Q.rq, Q.lv, Q.wo[3], lw);
  out_tile<9, 9>(p, ST, Q, Wf, mg, lds, w, lane, mt, n2, tag);
}

__global__ __launch_bounds__(512, 2) void fused_block(Params p0) {
  Stamps ST;
  stamp(9);
  sstamp(29);
  const int lane = threadIdx.x & 63;
  const int b = blockIdx.x, xcd = b & 7, i = b >> 3, par = xcd & 1, cgp = xcd >> 1;
  {
    u64 pc; asm volatile("s_getpc_b64 %0" : "=s"(pc));
    const unsigned o = min((unsigned)(i * 16 + (lane & 15)) * 128u, (unsigned)CODE_KB * 1024u);
    asm volatile("s_mov_b32 m0, %2\n\ts_nop 0\n\tglobal_load_lds_dword %0, %1" :: "v"(o), "s"(pc), "i"(LDS_BYTES - 256) : "memory");
  }
  Params p = p0;
  asm volatile("" : "+s"(p.x), "+s"(p.wqkv), "+s"(p.wo), "+s"(p.w1), "+s"(p.w3), "+s"(p.w2), "+s"(p.out), "+s"(p.ws));
  extern __shared__ __attribute__((aligned(16))) char smem[];
  const unsigned lds = (unsigned)(size_t)(LDSP char*)smem;
  const int w = rfl(threadIdx.x >> 6);
  sstamp(30);
  stamp(0);
  u64 tag;
  const int mt = xcd * 4 + (i >> 3), n2 = i & 7;
  if (i < 18) { const int u = 18 * cgp + i; ffn_tile(p, ST, lds, b, w, lane, par, u & 7, u >> 3, mt, n2, (par * 8 + (u & 7)) * 9 + (u >> 3), tag); }
  else { const int a = i - 18, hi = a >= 7, jt = a - 7 * hi, hg = 2 * cgp + hi; att_tile(p, ST, lds, b, w, lane, par, jt, hg, a * 8 + xcd, mt, n2, 144 + (par * 7 + jt) * 8 + hg, tag); }
}

extern "C" void kernel_launch(void* const* d_in, const int* in_sizes, int n_in, void* d_out, int out_size, void* d_ws, size_t ws_size, hipStream_t stream) {
  Params p;
  p.x = (const char*)d_in[0]; p.wqkv = (const char*)d_in[1]; p.wo = (const char*)d_in[2];
  p.w1 = (const char*)d_in[4]; p.w3 = (const char*)d_in[5]; p.w2 = (const char*)d_in[6];
  p.out = (char*)d_out; p.ws = (char*)d_ws;
  (void)hipFuncSetAttribute((const void*)fused_block, hipFuncAttributeMaxDynamicSharedMemorySize, LDS_BYTES);
  fused_block<<<256, 512, LDS_BYTES, stream>>>(p);
}
```

```cpp
#include <hip/hip_runtime.h>
#include <hip/hip_bf16.h>
#include <utility>

typedef short    s16x4 __attribute__((ext_vector_type(4)));
typedef __bf16   bf16x2 __attribute__((ext_vector_type(2)));
typedef __bf16   bf16x8 __attribute__((ext_vector_type(8)));
typedef float    f32x2 __attribute__((ext_vector_type(2)));
typedef float    f32x4 __attribute__((ext_vector_type(4)));
typedef unsigned u32x2 __attribute__((ext_vector_type(2)));
typedef unsigned u32x4 __attribute__((ext_vector_type(4)));
typedef unsigned long long u64;
typedef u64 u64x8 __attribute__((ext_vector_type(8)));
typedef __amdgpu_buffer_rsrc_t rsrc_t;
#define LDSP __attribute__((address_space(3)))
#define GAS __attribute__((address_space(1)))
#define DI __device__ __forceinline__
#define GAP() __builtin_amdgcn_sched_barrier(0)
template <int V> using IC = std::integral_constant<int, V>;
template <class F, int... I> DI void sfor_(F&& f, std::integer_sequence<int, I...>) { (f(IC<I>{}), ...); }
template <int N, class F> DI void sfor(F&& f) { sfor_(f, std::make_integer_sequence<int, N>{}); }

#define STAMPS 0
#define ROT 0
#define DPOS 16
#define RPOS 0
#define ADPOS 11
#define ARPOS 0
#define NTOUCH 0
#define LTOUCH 0
#define JEARLY 1
#define NPRE 16
#define PORD 2
#define BAL 0
#define FCHK 14
#define NINL 10
#define NOLD 1
#define MIDST 1
#define MAP2 0
#define NYNG 2
#define ATR 3
#define KEEPW 8
#define APRE 12
#define PRIO 1
#define ABL 0

constexpr unsigned WS_E  = 0;
constexpr unsigned WS_FH = 4096;
constexpr unsigned WS_FW = 20480;
constexpr unsigned WS_GR = 32768;
constexpr unsigned WS_GC = 65536;
constexpr unsigned CTL_BYTES = WS_GC + 3u * 5376u * 8u;
constexpr unsigned WS_H = 256u << 10;
constexpr unsigned H_BYTES = 32u * 74u * 4096u;
constexpr unsigned WS_W = WS_H + H_BYTES;
constexpr unsigned W_BYTES = 8u * 74u * 5120u;
constexpr unsigned WS_ST = 32u << 20;
constexpr unsigned OOB = 0x7f000000u;
constexpr int LDS_BYTES = 163840;
constexpr int SC1 = 16;
constexpr int SPIN_CAP = 1 << 18;
constexpr int CODE_KB = 44;

struct Params {
  const char* x; const char* wqkv; const char* wo; const char* w1; const char* w3; const char* w2;
  char* out; char* ws;
};

DI Params args(u64x8& ka) {
  asm volatile("s_waitcnt lgkmcnt(0)" : "+s"(ka) :: "memory");
  const u64 a0 = ka[0], a1 = ka[1], a2 = ka[2], a3 = ka[3], a4 = ka[4], a5 = ka[5], a6 = ka[6], a7 = ka[7];
  Params p;
  p.x = (const char*)a0; p.wqkv = (const char*)a1; p.wo = (const char*)a2; p.w1 = (const char*)a3; p.w3 = (const char*)a4; p.w2 = (const char*)a5; p.out = (char*)a6; p.ws = (char*)a7;
  return p;
}
DI const char* sel(bool c, const char* a, const char* b) { return c ? a : b; }
DI unsigned rfl(unsigned v) { return __builtin_amdgcn_readfirstlane(v); }
DI rsrc_t mk(const void* p, unsigned bytes) { return __builtin_amdgcn_make_buffer_rsrc((void*)p, (short)0, (int)bytes, 0x00020000); }
DI void dma(rsrc_t r, unsigned voff, unsigned soff, unsigned lds) {
  asm volatile("s_mov_b32 m0, %0\n\ts_nop 0\n\tbuffer_load_dwordx4 %1, %2, %3 offen lds" :: "s"(lds), "v"(voff), "s"(r), "s"(soff) : "memory");
}
DI void dma_dev(rsrc_t r, unsigned voff, unsigned soff, unsigned lds) {
  asm volatile("s_mov_b32 m0, %0\n\ts_nop 0\n\tbuffer_load_dwordx4 %1, %2, %3 offen sc1 lds" :: "s"(lds), "v"(voff), "s"(r), "s"(soff) : "memory");
}
template <unsigned SI, unsigned LI> DI void dmai(rsrc_t r, unsigned voff, unsigned soff, unsigned lds) {
  unsigned t;
  asm volatile("s_add_u32 m0, %1, %5\n\ts_add_u32 %0, %4, %6\n\tbuffer_load_dwordx4 %2, %3, %0 offen lds" : "=&s"(t) : "s"(lds), "v"(voff), "s"(r), "s"(soff), "i"(LI), "i"(SI) : "memory", "scc");
}
template <unsigned SI, unsigned LI> DI void dmai_dev(rsrc_t r, unsigned voff, unsigned soff, unsigned lds) {
  unsigned t;
  asm volatile("s_add_u32 m0, %1, %5\n\ts_add_u32 %0, %4, %6\n\tbuffer_load_dwordx4 %2, %3, %0 offen sc1 lds" : "=&s"(t) : "s"(lds), "v"(voff), "s"(r), "s"(soff), "i"(LI), "i"(SI) : "memory", "scc");
}
DI void st128_dev(u32x4 d, rsrc_t r, unsigned voff, unsigned soff) {
  asm volatile("buffer_store_dwordx4 %0, %1, %2, %3 offen sc1\n\ts_nop 1" :: "v"(d), "v"(voff), "s"(r), "s"(soff) : "memory");
}
DI u32x4 hload4(rsrc_t r, unsigned voff, unsigned soff) { u32x4 v; asm volatile("buffer_load_dwordx4 %0, %1, %2, %3 offen" : "=v"(v) : "v"(voff), "s"(r), "s"(soff) : "memory"); return v; }
DI unsigned hload1(rsrc_t r, unsigned voff, unsigned soff) { unsigned v; asm volatile("buffer_load_dword %0, %1, %2, %3 offen" : "=v"(v) : "v"(voff), "s"(r), "s"(soff) : "memory"); return v; }
template <class T> DI void land(T& v) { asm volatile("" : "+v"(v) :: "memory"); }
template <int N> DI void wait_bar() {
  __builtin_amdgcn_s_waitcnt(0xC07F);
  asm volatile("s_waitcnt vmcnt(%0) lgkmcnt(0)\n\ts_barrier" :: "i"(N) : "memory");
}
template <int N> DI u64 first_bar(const char* e) {
  u64 v; asm volatile("s_load_dwordx2 %0, %1, 0x0\n\ts_waitcnt vmcnt(%2) lgkmcnt(0)\n\ts_barrier" : "=&s"(v) : "s"(e), "i"(N) : "memory"); return v;
}
DI void lds_bar() { asm volatile("s_waitcnt lgkmcnt(0)\n\ts_barrier" ::: "memory"); }
DI unsigned opaque(unsigned v) { asm volatile("" : "+v"(v)); return v; }
DI bf16x8 ld128(unsigned a) { return *(const LDSP bf16x8*)(size_t)a; }
DI u32x4  ld128u(unsigned a) { return *(const LDSP u32x4*)(size_t)a; }
DI f32x4  ld128f(unsigned a) { return *(const LDSP f32x4*)(size_t)a; }
DI u32x2  ld64(unsigned a) { return *(const LDSP u32x2*)(size_t)a; }
DI u32x2  ld64v(unsigned a) { return *(const volatile LDSP u32x2*)(size_t)a; }
DI f32x2  ld64f(unsigned a) { return *(const LDSP f32x2*)(size_t)a; }
DI float  ld32f(unsigned a) { return *(const LDSP float*)(size_t)a; }
DI u32x2  tr64(unsigned a) { s16x4 v = __builtin_amdgcn_ds_read_tr16_b64_v4i16((LDSP s16x4*)(size_t)a); return __builtin_bit_cast(u32x2, v); }
DI bf16x8 cat(u32x2 a, u32x2 b) { u32x4 v = {a.x, a.y, b.x, b.y}; return __builtin_bit_cast(bf16x8, v); }
DI bf16x8 asb(u32x4 v) { return __builtin_bit_cast(bf16x8, v); }
DI f32x4 mfma(bf16x8 a, bf16x8 b, f32x4 c) { return __builtin_amdgcn_mfma_f32_16x16x32_bf16(a, b, c, 0, 0, 0); }
DI void mac(f32x4& c, bf16x8 a, bf16x8 b) { asm volatile("v_mfma_f32_16x16x32_bf16 %0, %1, %2, %0" : "+v"(c) : "v"(a), "v"(b)); }
DI void mac0(f32x4& c, bf16x8 a, bf16x8 b) { asm volatile("v_mfma_f32_16x16x32_bf16 %0, %1, %2, 0" : "=&v"(c) : "v"(a), "v"(b)); }
DI void settle() { asm volatile("s_nop 7\n\ts_nop 1" ::: "memory"); GAP(); }
DI unsigned pk(float lo, float hi) { f32x2 v = {lo, hi}; bf16x2 b = __builtin_convertvector(v, bf16x2); return __builtin_bit_cast(unsigned, b); }
DI float asf(unsigned u) { return __builtin_bit_cast(float, u); }
DI unsigned asu(float f) { return __builtin_bit_cast(unsigned, f); }
DI f32x2 fma2(f32x2 a, f32x2 b, f32x2 c) { return __builtin_elementwise_fma(a, b, c); }
DI f32x2 lo2(f32x4 v) { return __builtin_shufflevector(v, v, 0, 1); }
DI f32x2 hi2(f32x4 v) { return __builtin_shufflevector(v, v, 2, 3); }
DI f32x2 bc2(float v) { return (f32x2){v, v}; }
DI unsigned pk2(f32x2 v) { bf16x2 b = __builtin_convertvector(v, bf16x2); return __builtin_bit_cast(unsigned, b); }
DI float max3(float a, float b, float c) { float r; asm("v_max3_f32 %0, %1, %2, %3" : "=v"(r) : "v"(a), "v"(b), "v"(c)); return r; }
DI float max2(float a, float b) { float r; asm("v_max_f32 %0, %1, %2" : "=v"(r) : "v"(a), "v"(b)); return r; }
DI float blo(unsigned u) { return asf(u << 16); }
DI float bhi(unsigned u) { return asf(u & 0xffff0000u); }
DI float swadd32(float a, float b) { auto r = __builtin_amdgcn_permlane32_swap(asu(a), asu(b), false, false); unsigned r0 = r[0], r1 = r[1]; return asf(r0) + asf(r1); }
DI float swadd16(float a, float b) { auto r = __builtin_amdgcn_permlane16_swap(asu(a), asu(b), false, false); unsigned r0 = r[0], r1 = r[1]; return asf(r0) + asf(r1); }
template <int C> DI float dppadd(float v) { return v + asf(__builtin_amdgcn_update_dpp(0u, asu(v), C, 0xf, 0xf, true)); }
DI float rsum4(float v) { v = swadd16(v, v); return swadd32(v, v); }
DI float wsum(float v) { v = dppadd<0xB1>(v); v = dppadd<0x4E>(v); v = dppadd<0x141>(v); v = dppadd<0x128>(v); return rsum4(v); }
DI float rmax4(float v) {
  auto a = __builtin_amdgcn_permlane16_swap(asu(v), asu(v), false, false); unsigned a0 = a[0], a1 = a[1]; v = max2(asf(a0), asf(a1));
  auto b = __builtin_amdgcn_permlane32_swap(asu(v), asu(v), false, false); unsigned b0 = b[0], b1 = b[1]; return max2(asf(b0), asf(b1));
}
DI u32x4 halffrag(u32x2 a, u32x2 b) {
  auto p = __builtin_amdgcn_permlane32_swap(a.x, b.x, false, false);
  auto q = __builtin_amdgcn_permlane32_swap(a.y, b.y, false, false);
  unsigned p0 = p[0], p1 = p[1], q0 = q[0], q1 = q[1];
  return (u32x4){p0, q0, p1, q1};
}
DI void poll(const char* addr, u64 tag) {
#pragma unroll 1
  for (int i = 0; i < SPIN_CAP; ++i) {
    u32x2 v; asm volatile("global_load_dwordx2 %0, %1, off sc1\n\ts_waitcnt vmcnt(0)" : "=&v"(v) : "v"(addr) : "memory");
    const unsigned lo = v.x, hi = v.y;
    if (__all(lo == (unsigned)tag && hi == (unsigned)(tag >> 32))) break;
    __builtin_amdgcn_s_sleep(1);
  }
}
DI void put_flag(char* addr, u64 tag) { __hip_atomic_store((GAS u64*)addr, tag, __ATOMIC_RELAXED, __HIP_MEMORY_SCOPE_AGENT); }
struct Stamps {};
#define stamp(slot) do {} while (0)
#define cstamp(f) do {} while (0)
#define dump_sv() do {} while (0)
#define sstamp(j) do {} while (0)

struct Job {
  rsrc_t rc; unsigned cv, cstride;
  u32x4 c[4], xa, sel; unsigned xb, dst; f32x4 d; float s, q;
};
DI void job_setup(Job& J, const Params& p, int b, int w, int lane) {
  const int grp = b / 3, th = b - 3 * grp;
  const bool q = grp < 30, one = grp < 57;
  J.cstride = q ? 3840u : 3456u;
  J.rc = mk(sel(q, p.wqkv, sel(one, p.w1, p.w3)), grp < 84 ? 640u * J.cstride : 0u);
  J.cv = __umul24(8 * (3 * w + th) + (lane >> 3), J.cstride) + (unsigned)((q ? grp : one ? grp - 30 : grp - 57) * 128 + (lane & 7) * 16);
}
template <int I> DI void job_loadc(Job& J) { J.c[I] = hload4(J.rc, J.cv, I * 192u * J.cstride); }
DI void job_loadx(Job& J, rsrc_t rx, int b, int w, int lane) {
  const int v = 8 * b + w; const unsigned so = (unsigned)(2 * (v & 1023) + (v >> 10)) * 1280u;
  J.xa = hload4(rx, lane * 16u, so); J.xb = hload1(rx, 1024u + lane * 4u, so);
}
template <class T> DI void pin(T& v) { asm volatile("" : "+v"(v)); }
DI void job_prep(Job& J, unsigned part  , int w, int lane) {
  const int t = lane & 15; const unsigned one = (t & 1) ? 0x3f800000u : 0x00003f80u;
  J.sel = (u32x4){(t >> 1) == 0 ? one : 0u, (t >> 1) == 1 ? one : 0u, (t >> 1) == 2 ? one : 0u, (t >> 1) == 3 ? one : 0u};
  J.dst = part + (lane < 32 ? w * 256u : 2048u) + (lane & 7) * 32u + (lane & 16);
  pin(J.sel); pin(J.dst);
}
template <int C> DI float dppadd1(float v) { asm volatile("v_add_f32_dpp %0, %0, %0 row_ror:%1 row_mask:0xf bank_mask:0xf" : "+v"(v) : "i"(C)); return v; }
template <int P> DI void job_cols(Job& J) {
  if constexpr (P == 0) mac0(J.d, asb(J.sel), asb(J.c[0]));
  else if constexpr (P < 4) mac(J.d, asb(J.sel), asb(J.c[P]));
  else if constexpr (P == 9) J.d.x = dppadd1<8>(J.d.x);
  else if constexpr (P == 10) J.d.y = dppadd1<8>(J.d.y);
  else if constexpr (P == 11) J.d.z = dppadd1<8>(J.d.z);
  else if constexpr (P == 12) J.d.w = dppadd1<8>(J.d.w);
  else if constexpr (P == 13) *(LDSP f32x4*)(size_t)J.dst = J.d;
}
DI float dot2(unsigned a, unsigned b, float c) { return __builtin_amdgcn_fdot2_f32_bf16(__builtin_bit_cast(bf16x2, a), __builtin_bit_cast(bf16x2, b), c, false); }
template <int P> DI void job_rows(Job& J, rsrc_t rctl, int b, int w, int lane, unsigned tag) {
  if constexpr (P < 5) {
    const unsigned a = P == 0 ? J.xa.x : P == 1 ? J.xa.y : P == 2 ? J.xa.z : P == 3 ? J.xa.w : J.xb;
    J.s = dot2(a, 0x3f803f80u, P == 0 ? 0.f : J.s); J.q = dot2(a, a, P == 0 ? 0.f : J.q); pin(J.s); pin(J.q);
  } else if constexpr (P == 6) { J.s = swadd32(J.s, J.q); pin(J.s); }
  else if constexpr (P == 8) J.s = dppadd1<1>(J.s);
  else if constexpr (P == 9) J.s = dppadd1<2>(J.s);
  else if constexpr (P == 10) J.s = dppadd1<4>(J.s);
  else if constexpr (P == 11) J.s = dppadd1<8>(J.s);
  else if constexpr (P == 13) { J.s = swadd16(J.s, J.s); pin(J.s); }
  else if constexpr (P == 14) __builtin_amdgcn_raw_buffer_store_b64((u32x2){asu(J.s), tag}, rctl, (lane & 31) ? OOB : lane >> 2, WS_GR + (8 * b + w) * 16u, SC1);
}
DI u32x2 regrab(const char* src, unsigned tag) {
  u32x2 v = {0u, 0u};
#pragma unroll 1
  for (int i = 0; i < SPIN_CAP; ++i) {
    asm volatile("global_load_dwordx2 %0, %1, off sc1\n\ts_waitcnt vmcnt(0)" : "=&v"(v) : "v"(src) : "memory");
    const unsigned tg = v.y;
    if (__all(tg == tag)) break;
    __builtin_amdgcn_s_sleep(1);
  }
  return v;
}
DI float colsum(unsigned a, unsigned st, const char* ws, unsigned col, unsigned tag) {
  u32x2 g0 = ld64(a), g1 = ld64(a + st), g2 = ld64(a + 2 * st);
  const unsigned t0 = g0.y, t1 = g1.y, t2 = g2.y;
  if (!__all(t0 == tag && t1 == tag && t2 == tag)) { const char* src = ws + WS_GC + col * 8u; g0 = regrab(src, tag); g1 = regrab(src + 5376 * 8, tag); g2 = regrab(src + 2 * 5376 * 8, tag); }
  const unsigned v0 = g0.x, v1 = g1.x, v2 = g2.x;
  return asf(v0) + asf(v1) + asf(v2);
}
DI f32x2 rowstat(unsigned a, const char* ws, unsigned row, unsigned tag) {
  const u32x4 v = ld128u(a);
  unsigned s = v.x, q = v.z; const unsigned t0 = v.y, t1 = v.w;
  if (!__all(t0 == tag && t1 == tag)) { const char* src = ws + WS_GR + row * 16u; const u32x2 g0 = regrab(src, tag), g1 = regrab(src + 8, tag); s = g0.x; q = g1.x; }
  const float m = asf(s) * (1.f / 640.f);
  return (f32x2){m, __builtin_amdgcn_rsqf(fmaxf(fmaf(asf(q), 1.f / 640.f, -m * m), 0.f) + 1e-5f)};
}
DI void copy_wflags(rsrc_t rctl, unsigned dst, int half, int lane) { dma_dev(rctl, (half == 0 || lane < 10) ? lane * 128u : OOB, WS_FW + half * 8192u, dst + half * 1024u); }
DI void check_wflags(const Params& p, unsigned src, int lane, u64 tag) {
  const u32x2 f = ld64(src + lane * 16u), e = ld64(src + (lane < 10 ? 1024u + lane * 16u : 0u));
  const unsigned f0 = f.x, f1 = f.y, e0 = e.x, e1 = e.y, lo = (unsigned)tag, hi = (unsigned)(tag >> 32);
  if (!__all(f0 == lo && f1 == hi && e0 == lo && e1 == hi)) { poll(p.ws + WS_FW + lane * 128u, tag); poll(p.ws + WS_FW + (lane < 10 ? 64 + lane : 0) * 128u, tag); }
}
DI void job_pubc(unsigned part, rsrc_t rctl, int b, int lane, unsigned tag) {
  float s = 0.f;
#pragma unroll
  for (int k = 0; k < 8; ++k) s += ld32f(part + k * 256u + lane * 4u);
  const int grp = b / 3, th = b - 3 * grp;
  __builtin_amdgcn_raw_buffer_store_b64((u32x2){asu(s), tag}, rctl, grp < 84 ? lane * 8u : OOB, WS_GC + (unsigned)(th * 5376 + grp * 64) * 8u, SC1);
}

struct P2 { rsrc_t rq; unsigned st[10], wo[10], lv; };
DI void p2_setup(P2& Q, int n, int w) {
  const int hs = w < 2 ? 20 + 7 * w : w < 4 ? 22 + 6 * w : 18 + 7 * w;
#pragma unroll
  for (int s = 0; s < 6; ++s) Q.st[s] = hs + s;
  Q.st[6] = (w & 6) == 2 ? 16 + w : hs + 6; Q.st[7] = 2 * w; Q.st[8] = 2 * w + 1;
  Q.st[9] = w < 2 ? 16 + w : 0x10000;
#pragma unroll
  for (int s = 0; s < 10; ++s) Q.wo[s] = (n * 74u + Q.st[s]) * 5120u;
}

enum { EV_NONE = 0, EV_W = 1000  , EV_X = 300  , EV_P = 500  ,
       EV_JC = 10  , EV_JX = 14, EV_FCOPY = 16, EV_GCOPY = 17, EV_TDMA = 18, EV_TST = 19, EV_R = 600   };
struct Sched { int n; int j[300], m[300], c[300]; };
constexpr int J_ROWS = PORD >= 6 ? 3 : PORD ? 2 : JEARLY ? 3 : 6, J_COLS = PORD >= 6 ? 5 : PORD ? 4 : JEARLY ? 8 : 9, J_PUB = J_COLS + 1, J_GRAN = 17, F_CHK = FCHK, A_TR = ATR, A_FLAG = ATR + 4, A_ROWS = PORD >= 6 ? 8 : PORD ? 2 : 6, A_COLS = ATR + 2  , A_CHK = 14;
constexpr Sched make_sched(bool ffn) {
  Sched S{}; int n = 0;
  const int NW = ffn ? 3 : 2;
  auto put = [&](int j, int m, int c) { S.j[n] = j; S.m[n] = m; S.c[n] = c; ++n; };
  for (int j = -1; j < 20; ++j) {
    int L[24] = {}; int k = 0;
    auto W = [&](int s) { for (int i = 0; i < NW; ++i) { if (s < 20) L[k++] = EV_W + 4 * s + i; else if (ffn) L[k++] = EV_P + (s & 3) * 3 + i; } };
    auto X = [&](int s, int h) { if (s < 10) L[k++] = EV_X + 2 * s + h; else if (ffn) L[k++] = EV_P + 12 + (s % 3) * 2 + h; };
    auto E = [&](int c) { L[k++] = c; };
    if (j == -1) {
      if (PORD == 0) { W(0); X(0, 0); X(0, 1); W(1); W(2); X(1, 0); X(1, 1); }
      if (PORD == 1) { E(EV_JC); W(0); X(0, 0); X(0, 1); E(EV_JX); W(1); E(EV_JC + 1); W(2); X(1, 0); X(1, 1); E(EV_JC + 2); E(EV_JC + 3); }
      if (PORD == 2) { W(0); X(0, 0); X(0, 1); E(EV_JC); E(EV_JX); W(1); E(EV_JC + 1); W(2); X(1, 0); X(1, 1); E(EV_JC + 2); E(EV_JC + 3); }
      if (PORD == 3) { E(EV_JC); E(EV_JX); E(EV_JC + 1); E(EV_JC + 2); E(EV_JC + 3); W(0); X(0, 0); X(0, 1); W(1); W(2); X(1, 0); X(1, 1); }
      if (PORD == 4) { E(EV_JC); W(0); X(0, 0); X(0, 1); W(1); E(EV_JX); E(EV_JC + 1); E(EV_JC + 2); E(EV_JC + 3); W(2); X(1, 0); X(1, 1); }
      if (PORD == 5) { W(0); X(0, 0); X(0, 1); W(1); W(2); X(1, 0); X(1, 1); E(EV_JC); E(EV_JX); E(EV_JC + 1); E(EV_JC + 2); E(EV_JC + 3); }
      if (PORD == 6) { W(0); X(0, 0); X(0, 1); E(EV_JC); W(1); }
      if (PORD == 7) { W(0); X(0, 0); X(0, 1); W(1); E(EV_JC); }
      if (PORD == 8) { W(0); X(0, 0); X(0, 1); E(EV_JC); E(EV_JX); W(1); }
    }
    else if (PORD >= 6 && j == 0) { W(2); X(1, 0); X(1, 1); if (PORD != 8) E(EV_JX); E(EV_JC + 1); W(3); }
    else if (PORD >= 6 && j == 1) { X(2, 0); X(2, 1); W(4); E(EV_JC + 2); W(5); X(3, 0); }
    else if (PORD >= 6 && j == 2) { E(EV_JC + 3); W(6); X(3, 1); }
    else if (j == 0) { W(3); X(2, 0); X(2, 1); }
    else if (j == 1) { W(4); W(5); X(3, 0); }
    else { W(j + 4); if (j % 2 == 0) X(j / 2 + 2, 1); else X((j - 1) / 2 + 3, 0); }
    if (ffn && j == 19) L[k++] = EV_P + 13;
    const int k0 = k;
    if (ffn && j > F_CHK) for (int i = (j - F_CHK - 1) * NINL / (19 - F_CHK); i < (j - F_CHK) * NINL / (19 - F_CHK); ++i) put(j, 3 + 6 * (i & 1), EV_R + 20 + i);
    if (!ffn) {
      if (j == 15) { L[k++] = EV_P + 15; }
      if (j == 16) { L[k++] = EV_P + 16; L[k++] = EV_P + 17; L[k++] = EV_P + 10; L[k++] = EV_P + 11; }
      if (j == 17) { L[k++] = EV_P + 12; L[k++] = EV_P + 13; L[k++] = EV_P + 14; L[k++] = EV_R + 20; }
      if (j == 18) for (int i = 21; i < 25; ++i) L[k++] = EV_R + i;
      if (j == 19) for (int i = 6; i < 10; ++i) L[k++] = EV_P + i;
    }
    const int last = ffn ? 23 : 14;
    for (int i = 0; i < k; ++i) put(j, j == -1 ? 0 : (PORD >= 6 && j < 2) ? (ffn ? 1 + 2 * i : 1 + i + i / 2) : j <= 1 ? (ffn ? 4 + 3 * i - (i > 5) : 2 + 2 * i) : ffn ? (k > 4 && DPOS > 14 ? 14 : DPOS) + 2 * i : (j >= 15 ? 2 + 3 * i : ADPOS + i), L[i]);
    if (PORD) {} else if (JEARLY) {
      if (j == -1) { put(j, 0, EV_JC); put(j, 0, EV_JX); }
      if (j == 1) put(j, last, EV_JC + 1);
      if (j == 3) put(j, last, EV_JC + 2);
      if (j == 5) put(j, last, EV_JC + 3);
    } else {
      if (j == 1) put(j, last, EV_JC);
      if (j == 3) { put(j, last, EV_JX); put(j, last, EV_JC + 1); }
      if (j == 5) put(j, last, EV_JC + 2);
      if (j == 6) put(j, last, EV_JC + 3);
    }
    if (j == (ffn ? F_CHK : A_CHK) - 3) put(j, last, EV_FCOPY);
    if (j == J_GRAN - 3) put(j, last, EV_GCOPY);
    if (!ffn && j == (PORD >= 6 ? 1 : 0)) put(j, last, EV_TDMA);
    if (!ffn && j == A_TR + 1) put(j, 15, EV_TST);
  }
  for (int a = 1; a < n; ++a)
    for (int c = a; c > 0 && S.j[c - 1] == S.j[c] && S.m[c - 1] > S.m[c]; --c) {
      const int tm = S.m[c], tc = S.c[c]; S.m[c] = S.m[c - 1]; S.c[c] = S.c[c - 1]; S.m[c - 1] = tm; S.c[c - 1] = tc; }
  S.n = n; return S;
}
constexpr int ev_ops(int c, bool v7) {
  if (c >= EV_W) return (v7 && (c - EV_W) % 4 == 1) ? 0 : 1;
  if (c >= EV_P) return 1;
  if (c >= EV_X) return v7 ? 2 : 1;
  if (c >= EV_JC && c < EV_JX) return 1;
  return c == EV_JX ? 2 : 0;
}
constexpr int ev_due(int c, bool ffn) {
  if (c >= EV_W) return (c - EV_W) / 4 < 2 ? -1 : (c - EV_W) / 4 - 1;
  if (c >= EV_P) return 99;
  if (c >= EV_X) return 2 * ((c - EV_X) / 2) - 1;
  if (c >= EV_JC && c < EV_JX) return J_COLS;
  return c == EV_JX ? J_ROWS : c == EV_FCOPY ? (ffn ? F_CHK : A_CHK) : c == EV_GCOPY ? J_GRAN : c == EV_TDMA ? A_TR : c == EV_TST ? A_FLAG : 99;
}
constexpr int wait_n(const Sched& S, bool ffn, bool v7, int j) {
  int last = -1, end = 0;
  for (int i = 0; i < S.n; ++i) if (S.j[i] < j || (j == -1 && S.j[i] == -1)) { end = i + 1; if (ev_due(S.c[i], ffn) == j) last = i; }
  if (last < 0) return 63;
  int n = 0; for (int i = last + 1; i < end; ++i) n += ev_ops(S.c[i], v7);
  return n > 63 ? 63 : n;
}
constexpr Sched F_S = make_sched(true), A_S = make_sched(false);
constexpr int waitF(int j) { return wait_n(F_S, true, false, j); }
constexpr int waitA(int j) { return wait_n(A_S, false, false, j) < wait_n(A_S, false, true, j) ? wait_n(A_S, false, false, j) : wait_n(A_S, false, true, j); }
constexpr int ev_at(const Sched& S, int j, int m, int k) {
  for (int i = 0; i < S.n; ++i) if (S.j[i] == j && S.m[i] == m) { if (k == 0) return S.c[i]; --k; }
  return EV_NONE;
}

template <unsigned IMM> DI void ldev(u32x4& d, rsrc_t r, unsigned voff, unsigned soff) {
  asm volatile("buffer_load_dwordx4 %0, %1, %2, %3 offen offset:%4 sc1" : "=v"(d) : "v"(voff), "s"(r), "s"(soff), "i"(IMM) : "memory");
}
template <int F> DI void loadW(u32x4 (&Wf)[50], const P2& Q) {
  if constexpr (F % 5 < 4) ldev<(F % 5) * 1024u>(Wf[F], Q.rq, Q.lv, Q.wo[F / 5]); else ldev<3072u>(Wf[F], Q.rq, Q.lv, Q.wo[F / 5] + 1024u);
}
template <int S, int NF, int KEEP> DI void loadW_if(unsigned go, u32x4 (&Wf)[50], const P2& Q) {
  unsigned t;
  if constexpr (NF == 5)
    asm volatile("s_cmp_eq_u32 %9, 0\n\ts_cbranch_scc1 1f\n\ts_add_u32 %5, %8, 0x400\n\t"
                 "buffer_load_dwordx4 %0, %6, %7, %8 offen sc1\n\tbuffer_load_dwordx4 %1, %6, %7, %8 offen offset:1024 sc1\n\tbuffer_load_dwordx4 %2, %6, %7, %8 offen offset:2048 sc1\n\t"
                 "buffer_load_dwordx4 %3, %6, %7, %8 offen offset:3072 sc1\n\tbuffer_load_dwordx4 %4, %6, %7, %5 offen offset:3072 sc1\n\ts_waitcnt vmcnt(%10)\n1:"
                 : "+v"(Wf[5 * S]), "+v"(Wf[5 * S + 1]), "+v"(Wf[5 * S + 2]), "+v"(Wf[5 * S + 3]), "+v"(Wf[5 * S + 4]), "=&s"(t)
                 : "v"(Q.lv), "s"(Q.rq), "s"(Q.wo[S]), "s"(go), "i"(KEEP) : "memory", "scc");
  else
    asm volatile("s_cmp_eq_u32 %6, 0\n\ts_cbranch_scc1 1f\n\ts_add_u32 %2, %5, 0x400\n\t"
                 "buffer_load_dwordx4 %0, %3, %4, %5 offen offset:3072 sc1\n\tbuffer_load_dwordx4 %1, %3, %4, %2 offen offset:3072 sc1\n\ts_waitcnt vmcnt(%7)\n1:"
                 : "+v"(Wf[5 * S + 3]), "+v"(Wf[5 * S + 4]), "=&s"(t) : "v"(Q.lv), "s"(Q.rq), "s"(Q.wo[S]), "s"(go), "i"(KEEP) : "memory", "scc");
}
template <class T> DI void fresh(T& v) { asm volatile("" : "=v"(v)); }
template <int N> DI void waitv() { asm volatile("s_waitcnt vmcnt(%0)" :: "i"(N) : "memory"); }
template <int S0, int S1> DI void out_tile(const Params& p, Stamps& ST, const P2& Q, u32x4 (&Wf)[50], unsigned late, unsigned lds, int w, int lane, int mt, int n, u64 tag) {
  const rsrc_t rh = mk(p.ws + WS_H, H_BYTES);
  const int t = lane & 15, g = lane >> 4;
  const rsrc_t rx = mk(p.x, 2048u * 1280u), ro = mk(p.out, 2048u * 1280u);
  stamp(3); sstamp(34);
  const int b = blockIdx.x;
  const bool last = blockIdx.x == 0 && w >= 4;
  unsigned ov[2]; u32x4 xr[2];
  unsigned is3; asm("s_cmp_eq_u32 %1, 3\n\ts_cselect_b32 %0, 1, 0" : "=s"(is3) : "s"(w) : "scc");
  sfor<S1 - S0>([&](auto K) { loadW_if<S0 + K.value, 5, 63>(late, Wf, Q); });
  sfor<9 - S1>([&](auto K) { loadW_if<S1 + K.value, 5, 63>(w ^ 3, Wf, Q); });
  if (w == 3) poll(p.ws + WS_FH + ((mt >> 1) * 9 + (lane < 9 ? lane : 0)) * 64u, tag);
  sfor<9 - S1>([&](auto K) { loadW_if<S1 + K.value, 5, 63>(is3, Wf, Q); });
  const int jl = (mt & 15) ? (4 * (mt & 15) - 1) / 9 : 0, jh = (4 * (mt & 15) + 2) / 9;
  const unsigned fa = WS_FH + (144 + ((mt >> 4) * 7 + jl) * 8) * 64u, fn = (jh - jl + 1) * 8;
  u32x4 Hf[3][4], HX[4];
  constexpr auto bi = [](int q) { return q < 6 ? (q & 1) : (q - 4) % 3; };
  auto loadH = [&](auto Qp, auto I) { ldev<I.value * 1024u>(Hf[bi(Qp.value)][I.value], rh, Q.lv, (mt * 74u + Q.st[Qp.value]) * 4096u); };
  const unsigned lq = opaque(lds + w * 1024u + Q.lv), lq2 = opaque(lq + 8 * 8192u), lq3 = opaque(lq + 16 * 8192u);
  auto row = [&](int r) { return (r < 8 ? lq : r < 16 ? lq2 : lq3) + (r & 7) * 8192u; };
  const unsigned two = 2 - min(w, 2);
  lds_bar();
  stamp(4); stamp(5); sstamp(35);
  sfor<4>([&](auto I) { loadH(IC<0>{}, I); }); sfor<4>([&](auto I) { loadH(IC<1>{}, I); });
  f32x4 acc[5][4];
  constexpr auto nX = [](int q) { return q == 1 ? 1 : q == 5 ? 1 : 0; };
  sfor<7>([&](auto Qp) {
    constexpr int q = Qp.value;
    waitv<(q >= 2 ? nX(q - 2) : 0) + 4 + (q >= 1 ? nX(q - 1) : 0)>();
    sstamp(36 + q);
    land(Hf[bi(q)][0]); land(Hf[bi(q)][1]); land(Hf[bi(q)][2]); land(Hf[bi(q)][3]);
    if constexpr (q == 4) {
      const u32x2 f = ld64(lq); const unsigned f0 = f.x, f1 = f.y, l4 = opaque(Q.lv) >> 4;
      if (!__all(l4 >= fn || (f0 == (unsigned)tag && f1 == (unsigned)(tag >> 32)))) poll(p.ws + fa + (l4 < fn ? l4 : 0) * 64u, tag);
    }
    bf16x8 wf[5];
    auto get = [&](auto C) { constexpr int f = q * 5 + C.value;
      if constexpr (f >= 20) { land(Wf[f]); wf[C.value] = asb(Wf[f]); } else wf[C.value] = ld128(row(f)); };
    get(IC<0>{}); get(IC<1>{});
    sfor<5>([&](auto C) {
      if constexpr (C.value < 3) get(IC<C.value + 2>{});
#pragma unroll
      for (int a = 0; a < 4; ++a) { if constexpr (q == 0) mac0(acc[C.value][a], wf[C.value], asb(Hf[0][a])); else mac(acc[C.value][a], wf[C.value], asb(Hf[bi(q)][a])); }
      if constexpr (q >= 4 && C.value < 4) loadH(IC<q + 2>{}, C);
    });
    if constexpr (q < 4) sfor<4>([&](auto I) { loadH(IC<q + 2>{}, I); });
    if constexpr (q == 1) { if (lane < fn) dma_dev(mk(p.ws, CTL_BYTES), Q.lv * 4u, fa, lds + w * 1024u); }
    if constexpr (q == 5) {
      const unsigned l2 = opaque(Q.lv) >> 4;
#pragma unroll
      for (int i = 0; i < 2; ++i) {
        const int cc = (w + 8 * i) * 4 + (l2 >> 4), tb = (cc * 26) >> 8, c8 = cc - tb * 10;
        const int B = 4 * mt + tb, tok = 2 * ((B & 63) * 16 + (l2 & 15)) + (B >> 6);
        ov[i] = (unsigned)(tok * 1280 + n * 160 + c8 * 16);
      }
      xr[0] = hload4(rx, ov[0], 0u); fresh(xr[1]);
      asm volatile("s_cmp_eq_u32 %3, 0\n\ts_cbranch_scc1 1f\n\tbuffer_load_dwordx4 %0, %1, %2, 0 offen\n1:" : "+v"(xr[1]) : "v"(ov[1]), "s"(rx), "s"(two) : "memory", "scc");
      if (last) dma_dev(mk(p.ws, CTL_BYTES), lane * 64u, WS_FH + (w - 4) * 4096u, lds + w * 1024u);
    }
    if constexpr (q == 5) {
      sfor<5>([&](auto K) { fresh(Wf[45 + K.value]); }); sfor<4>([&](auto K) { fresh(HX[K.value]); });
      loadW_if<9, 5, 63>(two, Wf, Q);
      asm volatile("s_cmp_eq_u32 %7, 0\n\ts_cbranch_scc1 1f\n\tbuffer_load_dwordx4 %0, %4, %5, %6 offen sc1\n\tbuffer_load_dwordx4 %1, %4, %5, %6 offen offset:1024 sc1\n\t"
                   "buffer_load_dwordx4 %2, %4, %5, %6 offen offset:2048 sc1\n\tbuffer_load_dwordx4 %3, %4, %5, %6 offen offset:3072 sc1\n1:"
                   : "+v"(HX[0]), "+v"(HX[1]), "+v"(HX[2]), "+v"(HX[3]) : "v"(Q.lv), "s"(rh), "s"((mt * 74u + Q.st[9]) * 4096u), "s"(two) : "memory", "scc");
    }
    GAP();
  });
  sstamp(43);
  waitv<4>();
  sfor<5>([&](auto C) { land(Wf[45 + C.value]); }); sfor<4>([&](auto C) { land(HX[C.value]); });
  if (w < 2) sfor<5>([&](auto C) {
#pragma unroll
    for (int a = 0; a < 4; ++a) mac(acc[C.value][a], asb(Wf[45 + C.value]), asb(HX[a]));
  });
  GAP();
  waitv<3>();
  sstamp(44); sstamp(45);
  land(xr[0]); land(xr[1]);
  if (last) {
    const u32x2 f = ld64(lq); const unsigned f0 = f.x, f1 = f.y;
    if (!__all(f0 == (unsigned)tag && f1 == (unsigned)(tag >> 32))) poll(p.ws + WS_FH + ((w - 4) * 64 + lane) * 64u, tag);
  }
  sfor<10>([&](auto K) { land(Wf[35 + K.value]); });
  sfor<4>([&](auto A) {
    constexpr int a = A.value;
    if constexpr (a > 0) waitv<3 - a>();
    land(Hf[0][a]); land(Hf[1][a]);
    sfor<5>([&](auto C) { mac(acc[C.value][a], asb(Wf[35 + C.value]), asb(Hf[0][a])); GAP(); });
    sfor<5>([&](auto C) { mac(acc[C.value][a], asb(Wf[40 + C.value]), asb(Hf[1][a]));
      if constexpr (a > 0) *(LDSP f32x4*)(size_t)row(5 * (a - 1) + C.value) = acc[C.value][a - 1];
      GAP(); });
  });
  settle();
  stamp(6); sstamp(46);
#pragma unroll
  for (int c = 0; c < 5; ++c) *(LDSP f32x4*)(size_t)row(15 + c) = acc[c][3];
  lds_bar();
  if (last && w == 7 && lane == 0) put_flag(p.ws + WS_E, tag);
  stamp(7); sstamp(47);
#pragma unroll
  for (int i = 0; i < 2; ++i) {
    if (i == 0 || w < 2) {
      const int cc = (w + 8 * i) * 4 + g, tb = cc / 10, c8 = cc - tb * 10;
      const unsigned a = lds + (unsigned)(tb * 5 + (c8 >> 1)) * 8192u + (c8 & 1) * 512u + t * 16u;
      f32x4 lo[8], hi[8];
#pragma unroll
      for (int k = 0; k < 8; ++k) { lo[k] = ld128f(a + k * 1024u); hi[k] = ld128f(a + k * 1024u + 256u); }
      const f32x4 l = ((lo[0] + lo[1]) + (lo[2] + lo[3])) + ((lo[4] + lo[5]) + (lo[6] + lo[7])), hh = ((hi[0] + hi[1]) + (hi[2] + hi[3])) + ((hi[4] + hi[5]) + (hi[6] + hi[7]));
      const unsigned x0 = xr[i].x, x1 = xr[i].y, x2 = xr[i].z, x3 = xr[i].w;
      const u32x4 o = {pk(l.x + blo(x0), l.y + bhi(x0)), pk(l.z + blo(x1), l.w + bhi(x1)), pk(hh.x + blo(x2), hh.y + bhi(x2)), pk(hh.z + blo(x3), hh.w + bhi(x3))};
      __builtin_amdgcn_raw_buffer_store_b128(o, ro, ov[i], 0, 0);
    }
  }
  sstamp(48);
  dump_sv();
}

constexpr unsigned F_WS = 24576, F_XR = 98304, F_XS = 16384, F_EX = 147456;
constexpr unsigned FX_G = F_EX, FX_CS = F_EX + 11264, FX_ST = FX_CS + 1536, FX_FW = FX_ST + 1024, FX_JP = F_EX;
DI void ffn_tile(Stamps& ST, unsigned lds, int b, int w, int lane, int par, int rtl, int ct, int mt, int n2, int myflag, u64& tag) {
  u64x8 ka; asm volatile("s_load_dwordx16 %0, %1, 0x0" : "=s"(ka) : "s"(__builtin_amdgcn_kernarg_segment_ptr()) : "memory"); GAP();
  P2 Q;
  const int mg = w >> 2, ng = w & 3;
  const int t = lane & 15, g = lane >> 4;
  unsigned wv = (unsigned)(8 * ng + (lane >> 3)) * 3456u + (unsigned)((((lane & 7) >> 1) ^ (((lane >> 4) & 1) | ((ng & 1) << 1))) * 32 + (lane & 1) * 16);
  const unsigned ws0 = ct * 384u + mg * 128u, ws1 = ct * 384u + (mg ? 0u : 256u);
  unsigned xv = (unsigned)(2 * (8 * w + (lane >> 3))) * 1280u + (unsigned)(((lane & 7) ^ (((lane >> 4) & 3) | ((w & 1) << 2))) * 16);
  const unsigned xs0 = (unsigned)(2 * 128 * rtl + par) * 1280u;
  const unsigned lw = lds + w * 1024u;
  pin(wv); pin(xv);
  const Params p = args(ka);
  stamp(0); sstamp(30);
  const rsrc_t rx = mk(p.x, 2048u * 1280u), r1 = mk(p.w1, 640u * 3456u), r3 = mk(p.w3, 640u * 3456u);
  const rsrc_t rm = mk(sel(mg, p.w3, p.w1), 640u * 3456u), rctl = mk(p.ws, CTL_BYTES);
  Job J; u32x4 Wf[50];
  auto event = [&](auto C) {
    constexpr int c = C.value;
    if constexpr (c >= EV_W) {
      constexpr int s = (c - EV_W) / 4, i = (c - EV_W) % 4;
      dmai<s * 32u * 3456u + (i == 2 ? 128u : 0u), ((s & 3) * 3 + i) * 8192u>(i == 0 ? r1 : i == 1 ? rm : r3, wv, i == 1 ? ws1 : ws0, lw);
    } else if constexpr (c >= EV_R) loadW<c - EV_R>(Wf, Q);
    else if constexpr (c >= EV_P) dmai_dev<((c - EV_P) % 5) * 1024u, (c - EV_P) * 8192u>(Q.rq, Q.lv, Q.wo[(c - EV_P) / 5], lw);
    else if constexpr (c >= EV_X) {
      constexpr int s = (c - EV_X) / 2, h = (c - EV_X) % 2;
      dmai<s * 128u + h * (128u * 1280u), (12 + (s % 3) * 2 + h) * 8192u>(rx, xv, xs0, lw);
    } else if constexpr (c >= EV_JC && c < EV_JX) { if constexpr (c == EV_JC) job_setup(J, p, b, w, lane); job_loadc<c - EV_JC>(J); }
    else if constexpr (c == EV_JX) job_loadx(J, rx, b, w, lane);
    else if constexpr (c == EV_FCOPY) { if (w < 2) copy_wflags(rctl, lds + FX_FW, w, lane); }
    else if constexpr (c == EV_GCOPY) {
      if (w < 6) {
        const unsigned so = WS_GC + (unsigned)((w % 3) * 5376 + 1920 + (w / 3) * 1728 + 192 * ct) * 8u;
        dma_dev(rctl, Q.lv, so, lds + FX_G + w * 1536u);
        if (lane < 32) dma_dev(rctl, Q.lv, so + 1024u, lds + FX_G + w * 1536u + 1024u);
      } else dma_dev(rctl, Q.lv, WS_GR + (unsigned)(par * 1024 + rtl * 128 + (w - 6) * 64) * 16u, lds + FX_G + 9216u + (w - 6) * 1024u);
    }
  };
  sstamp(31);
  sfor<18>([&](auto K) { event(IC<ev_at(F_S, -1, 0, K.value)>{}); GAP(); });
  sstamp(32);
  Q.rq = mk(p.ws + WS_W, W_BYTES); Q.lv = lane * 16u;
  unsigned xa[2], wa[2][3];
  xa[0] = lds + F_XR + (unsigned)(64 * mg + t) * 128u + (unsigned)((g ^ (t >> 1)) * 16); xa[1] = opaque(xa[0] ^ 64u);
  {
    const int q = t >> 2, pp = t & 3, f = (q >> 1) | ((g & 1) << 1);
#pragma unroll
    for (int i = 0; i < 3; ++i) { const int fb = 3 * ng + i; wa[0][i] = lds + (fb >> 2) * 4096u + (8 * g + q) * 128u + (((fb & 3) ^ f) * 32) + 8 * pp; wa[1][i] = opaque(wa[0][i] + 2 * F_WS); }
  }
  f32x4 acc[6][4];
  bf16x8 X[2][4]; u32x2 W[2][12];
  auto rd = [&](auto S, auto R) {
    constexpr int s = S.value, r = R.value;
    if constexpr (r < 4) X[s & 1][r] = ld128(xa[s & 1] + ((s >> 1) % 3) * F_XS + r * 2048u);
    else { constexpr int k = r - 4, bb = k >> 1; W[s & 1][k] = tr64(wa[(s >> 1) & 1][bb % 3] + (s & 1) * F_WS + (bb / 3) * 12288u + (k & 1) * 512u); }
  };
  job_prep(J, lds + FX_JP, w, lane);
  tag = first_bar<waitF(-1)>(p.ws) + 1;
  stamp(1); cstamp(c0); sstamp(33);
  sfor<16>([&](auto R) { rd(IC<0>{}, R); });
  sfor<20>([&](auto JC) {
    constexpr int j = JC.value;
    if constexpr (j > 0) wait_bar<waitF(j)>();
    sstamp(j);
    constexpr int lead = (j == J_GRAN + 1 || j == F_CHK) ? 1 : 0;
    if constexpr (PRIO && lead) { if (mg) __builtin_amdgcn_s_setprio(1); }
    if constexpr (j == J_ROWS) { land(J.xa); land(J.xb); }
    if constexpr (j == J_COLS) { land(J.c[0]); land(J.c[1]); land(J.c[2]); land(J.c[3]); }
    if constexpr (j == F_CHK) { int w2 = w; asm volatile("" : "+s"(w2)); p2_setup(Q, n2, w2); }
    GAP();
    sfor<24>([&](auto MC) {
      constexpr int m = MC.value, bb = m / 4, a = m % 4;
      if constexpr (j == 0) mac0(acc[bb][a], cat(W[0][2 * bb], W[0][2 * bb + 1]), X[0][a]); else mac(acc[bb][a], cat(W[j & 1][2 * bb], W[j & 1][2 * bb + 1]), X[j & 1][a]);
      if constexpr (j < 19 && m >= RPOS && m < RPOS + 16) rd(IC<j + 1>{}, IC<m - RPOS>{});
      if constexpr (j == J_ROWS) job_rows<m>(J, rctl, b, w, lane, (unsigned)tag);
      if constexpr (j == J_COLS) job_cols<m>(J);
      sfor<3>([&](auto K) { event(IC<ev_at(F_S, j, m, K.value)>{}); });
      GAP();
    });
    if constexpr (j == J_PUB) { if (w == 1) job_pubc(lds + FX_JP, rctl, b, lane, (unsigned)tag); }
    if constexpr (j == F_CHK) { if (w == 7) check_wflags(p, lds + FX_FW, lane, tag); }
    if constexpr (j == J_GRAN || j == J_GRAN + 1) {
      if (mg == j - J_GRAN) {
        const int tid = w * 64 + lane;
        if (w < 6) {
          const int up = w >= 3, c = tid - 192 * up;
          *(LDSP float*)(size_t)(lds + FX_CS + tid * 4u) = colsum(lds + FX_G + up * 4608u + c * 8u, 1536u, p.ws, 1920 + up * 1728 + 192 * ct + c, (unsigned)tag);
        } else *(LDSP f32x2*)(size_t)(lds + FX_ST + (tid - 384) * 8u) = rowstat(lds + FX_G + 9216u + (tid - 384) * 16u, p.ws, par * 1024 + rtl * 128 + tid - 384, (unsigned)tag);
      }
    }
    if constexpr (PRIO && lead) __builtin_amdgcn_s_setprio(0);
    GAP();
  });
  settle();
  stamp(2); cstamp(c1); sstamp(20);
  f32x4 cG[3], cU[3]; f32x2 stt[4];
#pragma unroll
  for (int i = 0; i < 3; ++i) { cG[i] = ld128f(lds + FX_CS + ((3 * ng + i) * 16 + 4 * g) * 4u); cU[i] = ld128f(lds + FX_CS + 768u + ((3 * ng + i) * 16 + 4 * g) * 4u); }
#pragma unroll
  for (int a = 0; a < 4; ++a) stt[a] = ld64f(lds + FX_ST + (64 * mg + 16 * a + t) * 8u);
  const rsrc_t rh = mk(p.ws + WS_H, H_BYTES);
  const unsigned hv = (unsigned)(lane >> 5) * 1024u + (lane & 31) * 16u;
  const unsigned hs = (unsigned)(2 * (8 * par + rtl) + mg) * (74u * 4096u);
  sfor<25 - NINL>([&](auto K) { fresh(Wf[20 + NINL + K.value]); });
  sfor<NYNG>([&](auto K) { loadW_if<4 + NINL / 5 + K.value, 5, 63>(mg, Wf, Q); });
  GAP();
  f32x2 nm[4], k1[4], ik[4];
#pragma unroll
  for (int a = 0; a < 4; ++a) { nm[a] = bc2(-stt[a].x); k1[a] = bc2(stt[a].y * -1.44269504089f); ik[a] = bc2(__builtin_amdgcn_rcpf(stt[a].y * stt[a].y)); }
  sfor<6>([&](auto UU) {
    constexpr int uu = UU.value, ap = uu / 3, i = uu % 3;
    f32x2 G[4], U[4], E[4];
#pragma unroll
    for (int k = 0; k < 4; ++k) { const int a = 2 * ap + k / 2; G[k] = (k & 1) ? fma2(nm[a], hi2(cG[i]), hi2(acc[i][a])) : fma2(nm[a], lo2(cG[i]), lo2(acc[i][a])); }
    GAP();
#pragma unroll
    for (int k = 0; k < 4; ++k) E[k] = G[k] * k1[2 * ap + k / 2];
#pragma unroll
    for (int k = 0; k < 4; ++k) { const int a = 2 * ap + k / 2; U[k] = (k & 1) ? fma2(nm[a], hi2(cU[i]), hi2(acc[3 + i][a])) : fma2(nm[a], lo2(cU[i]), lo2(acc[3 + i][a])); }
    GAP();
#pragma unroll
    for (int k = 0; k < 4; ++k) E[k] = (f32x2){__builtin_amdgcn_exp2f(E[k].x), __builtin_amdgcn_exp2f(E[k].y)};
    GAP();
#pragma unroll
    for (int k = 0; k < 4; ++k) { E[k] = fma2(E[k], ik[2 * ap + k / 2], ik[2 * ap + k / 2]); G[k] = G[k] * U[k]; }
    GAP();
#pragma unroll
    for (int k = 0; k < 4; ++k) E[k] = (f32x2){__builtin_amdgcn_rcpf(E[k].x), __builtin_amdgcn_rcpf(E[k].y)};
    GAP();
#pragma unroll
    for (int k = 0; k < 4; ++k) G[k] = G[k] * E[k];
    GAP();
    const int fb = 12 * ct + 3 * ng + i;
    st128_dev(halffrag((u32x2){pk2(G[0]), pk2(G[1])}, (u32x2){pk2(G[2]), pk2(G[3])}), rh, hv, hs + (20 + (fb >> 1)) * 4096u + ap * 2048u + (fb & 1) * 512u);

  });
  sfor<NOLD>([&](auto K) { loadW_if<4 + NINL / 5 + K.value, 5, KEEPW>(1 - mg, Wf, Q); });
  sstamp(21);
  if (mg) wait_bar<0>(); else wait_bar<5 * NOLD>();
  sstamp(22); sstamp(23); sstamp(24); sstamp(25); sstamp(26); sstamp(27); sstamp(28);
  if (w == 0 && lane == 0) put_flag(p.ws + WS_FH + myflag * 64u, tag);
  dmai_dev<3072u, 18 * 8192u>(Q.rq, Q.lv, Q.wo[3], lw); dmai_dev<4096u, 19 * 8192u>(Q.rq, Q.lv, Q.wo[3], lw);
  out_tile<4 + NINL / 5 + (NOLD < NYNG ? NOLD : NYNG), 4 + NINL / 5 + (NOLD < NYNG ? NYNG : NOLD)>(p, ST, Q, Wf, NOLD < NYNG ? 1 - mg : mg, lds, w, lane, mt, n2, tag);
}

constexpr unsigned A_WS = 15360, A_XR = 61440, A_XS = 20480, A_JOB = 122880;
constexpr unsigned AX_G = 147456, AX_JP = AX_G, AX_FW = AX_G + 9216, AX_CS = AX_FW + 2048, AX_ST = AX_CS + 1024, AX_Z = AX_ST + 1536;
DI void att_tile(Stamps& ST, unsigned lds, int b, int w, int lane, int par, int jt, int hg, int job, int mt, int n2, int myflag, u64& tag) {
  u64x8 ka; asm volatile("s_load_dwordx16 %0, %1, 0x0" : "=s"(ka) : "s"(__builtin_amdgcn_kernarg_segment_ptr()) : "memory"); GAP();
  P2 Q;
  const int mg = w >> 2, h = (w + mg) & 3;
  const int t = lane & 15, g = lane >> 4;
  unsigned wv[2];
#pragma unroll
  for (int i = 0; i < 2; ++i) {
    const unsigned n = (2 * w + i) * 64 + lane, pos = (n * 2185u) >> 16, cc = n - pos * 30, sec = (cc * 26u) >> 8;
    const unsigned k = (pos & 19) | ((pos & 4) << 1) | ((pos & 8) >> 1);
    wv[i] = k * 3840u + sec * 1120u + cc * 16u;
  }
  const unsigned ws0 = hg * 160u;
  unsigned xv = (unsigned)(2 * (8 * w + (lane >> 3))) * 1280u + (unsigned)(((lane & 7) ^ (((lane >> 4) & 3) | ((w & 1) << 2))) * 16);
  const unsigned xs0 = (unsigned)(2 * 144 * jt + par) * 1280u;
  const bool isO = job < 20;
  const unsigned lw = lds + w * 1024u, lw2 = lds + w * 2048u;
  pin(wv[0]); pin(wv[1]); pin(xv);
  const Params p = args(ka);
  stamp(0); sstamp(30);
  const rsrc_t rx = mk(p.x, 2048u * 1280u), rw = mk(p.wqkv, 640u * 3840u), rctl = mk(p.ws, CTL_BYTES);
  Job J; u32x4 Wf[50];
  auto event = [&](auto C) {
    constexpr int c = C.value;
    if constexpr (c >= EV_W) {
      constexpr int s = (c - EV_W) / 4, i = (c - EV_W) % 4;
      if (i == 0 || w < 7) dmai<s * (32u * 3840u), (s & 3) * A_WS + i * 1024u>(rw, wv[i], ws0, lw2);
    } else if constexpr (c >= EV_R) loadW<c - EV_R>(Wf, Q);
    else if constexpr (c >= EV_P) dmai_dev<((c - EV_P) % 5) * 1024u, (c - EV_P) * 8192u>(Q.rq, Q.lv, Q.wo[(c - EV_P) / 5], lw);
    else if constexpr (c == EV_FCOPY) { if (w < 2) copy_wflags(rctl, lds + AX_FW, w, lane); }
    else if constexpr (c >= EV_X) {
      constexpr int s = (c - EV_X) / 2, hf = (c - EV_X) % 2;
      constexpr unsigned so = s * 128u + hf * (160u * 1280u), l = A_XR + (s % 3) * A_XS + hf * 10240u;
      dmai<so, l>(rx, xv, xs0, lw);
      if (w >= 6) dmai<so + 32u * 1280u, l + 2048u>(rx, xv, xs0, lw);
    } else if constexpr (c >= EV_JC && c < EV_JX) { if constexpr (c == EV_JC) job_setup(J, p, b, w, lane); job_loadc<c - EV_JC>(J); }
    else if constexpr (c == EV_JX) job_loadx(J, rx, b, w, lane);
    else if constexpr (c == EV_TDMA) {
      if (job < 74) {
        const rsrc_t rj = mk(sel(isO, p.wo, p.w2), isO ? 640u * 1280u : 1728u * 1280u);
        const int db = 2 * job + (w >> 2), q5 = db / 5, e = db - 5 * q5;
        const int u = !isO ? 32 * (job - 20) + 4 * w : e < 4 ? 20 * (4 * q5 + e) + 4 * (w & 3) : 20 * (4 * q5 + (w & 3)) + 16;
        const unsigned v = g * 1280u + (t ^ (2 * (g | ((w & 1) << 2)))) * 16u;
#pragma unroll
        for (int i = 0; i < 5; ++i) dma(rj, v, u * 1280u + i * 256u, lds + A_JOB + (5 * w + i) * 1024u);
      }
    } else if constexpr (c == EV_GCOPY) {
      if (lane < 40) {
        dma_dev(rctl, Q.lv, WS_GC + (unsigned)((w % 3) * 5376 + (w / 3) * 640 + 80 * hg) * 8u, lds + AX_G + w * 640u);
        if (w == 0) dma_dev(rctl, Q.lv, WS_GC + (unsigned)(2 * 5376 + 2 * 640 + 80 * hg) * 8u, lds + AX_G + 8 * 640u);
      }
      if (w >= 1 && w < 4) dma_dev(rctl, (w < 3 || lane < 32) ? Q.lv : OOB, WS_GR + (unsigned)(par * 1024 + jt * 144 + (w - 1) * 64) * 16u, lds + AX_G + 6144u + (w - 1) * 1024u);
    }
  };
  sstamp(31);
  sfor<15>([&](auto K) { event(IC<ev_at(A_S, -1, 0, K.value)>{}); GAP(); });
  sstamp(32);
  Q.rq = mk(p.ws + WS_W, W_BYTES); Q.lv = lane * 16u;
  unsigned xa[2], wm, wt;
  xa[0] = lds + A_XR + (unsigned)(80 * mg + t) * 128u + (unsigned)((g ^ (t >> 1)) * 16); xa[1] = opaque(xa[0] ^ 64u);
  {
    const int q = t >> 2, pp = t & 3;
    const unsigned rowb = lds + (unsigned)(q + 4 * (g & 1) + 16 * (g >> 1)) * 480u;
    wm = rowb + 40 * h + 8 * pp; wt = rowb + 40 * pp + 32 + h * 160;
  }
  f32x4 acc[4][5];
#pragma unroll
  for (int c = 0; c < 5; ++c) { acc[3][c] = (f32x4){0.f, 0.f, 0.f, 0.f}; pin(acc[3][c]); }
  bf16x8 X[2][5]; u32x2 W[2][8];
  auto rd = [&](auto S, auto R) {
    constexpr int s = S.value, r = R.value;
    if constexpr (r < 5) X[s & 1][r] = ld128(xa[s & 1] + ((s >> 1) % 3) * A_XS + r * 2048u);
    else { constexpr int k = r - 5; W[s & 1][k] = tr64((k < 6 ? wm + (k >> 1) * 160u : wt) + (s & 3) * A_WS + (k & 1) * 3840u); }
  };
  W[0][6] = W[0][7] = W[1][6] = W[1][7] = (u32x2){0u, 0u};
  bool vp[4]; float bias[4], bias0[4];
#pragma unroll
  for (int r = 0; r < 4; ++r) { const int kk = 4 * g + r; vp[r] = kk >= t + 9; const bool vc = kk <= t && kk >= t - 7;
    bias[r] = (vp[r] || vc) ? 0.f : -INFINITY; bias0[r] = ((vp[r] && mg) || vc) ? 0.f : -INFINITY; pin(bias[r]); pin(bias0[r]); }
  unsigned mp0 = (vp[0] ? 0xffffu : 0u) | (vp[1] ? 0xffff0000u : 0u), mp1 = (vp[2] ? 0xffffu : 0u) | (vp[3] ? 0xffff0000u : 0u);
  int dq = 4 * g - t - 9;
  pin(mp0); pin(mp1); pin(dq);
  const int B0 = par * 64 + 9 * jt;
  unsigned ov[4];
#pragma unroll
  for (int ap = 0; ap < 4; ++ap) {
    const int lb = (ap < 3 ? 5 * mg + 2 * ap : 2 * w) + (lane >> 5), B = B0 + lb;
    const bool ok = (lb > 0 || jt == 0) && (ap < 2 || (ap == 2 ? lane < 32 : w < 5));
    ov[ap] = ok ? (unsigned)(B >> 2) * (74u * 4096u) + (B & 3) * 1024u + (lane & 31) * 16u : OOB; pin(ov[ap]);
  }
  unsigned qz = g == h ? lds + 2048u + (unsigned)(5 * mg) * 4096u + t * 32u + g * 8u + 4 * 512u : lds + AX_Z, qs = g == h ? 4096u : 0u;
  pin(qz); pin(qs);
  job_prep(J, lds + AX_JP, w, lane);
  tag = first_bar<waitA(-1)>(p.ws) + 1;
  stamp(1); cstamp(c0); sstamp(33);
  sfor<11>([&](auto R) { rd(IC<0>{}, R); });
  if (h < 3) { rd(IC<0>{}, IC<11>{}); rd(IC<0>{}, IC<12>{}); }
  sfor<20>([&](auto JC) {
    constexpr int j = JC.value;
    if constexpr (j > 0) wait_bar<waitA(j)>();
    sstamp(j);
    if constexpr (j == A_FLAG) { if (job < 74 && w == 0 && lane == 0) put_flag(p.ws + WS_FW + job * 128u, tag); }
    constexpr int lead = (j == J_GRAN + 1 || j == A_TR + 1) ? 1 : 0;
    if constexpr (PRIO && lead) { if (mg) __builtin_amdgcn_s_setprio(1); }
    if constexpr (j == J_ROWS) { land(J.xa); land(J.xb); }
    if constexpr (j == J_COLS) { land(J.c[0]); land(J.c[1]); land(J.c[2]); land(J.c[3]); }
    if constexpr (j == A_CHK) { int w2 = w; asm volatile("" : "+s"(w2)); p2_setup(Q, n2, w2); }
    GAP();
    if (h < 3) {
      if constexpr (j < 19) { rd(IC<j + 1>{}, IC<11>{}); rd(IC<j + 1>{}, IC<12>{}); }
#pragma unroll
      for (int a = 0; a < 5; ++a) mac(acc[3][a], cat(W[j & 1][6], W[j & 1][7]), X[j & 1][a]);
    }
    GAP();
    sfor<15>([&](auto MC) {
      constexpr int m = MC.value, bb = m / 5, a = m % 5;
      if constexpr (j == 0) mac0(acc[bb][a], cat(W[0][2 * bb], W[0][2 * bb + 1]), X[0][a]); else mac(acc[bb][a], cat(W[j & 1][2 * bb], W[j & 1][2 * bb + 1]), X[j & 1][a]);
      if constexpr (j < 19 && m >= ARPOS && m < ARPOS + 11) rd(IC<j + 1>{}, IC<m - ARPOS>{});
      if constexpr (j == A_ROWS) job_rows<m>(J, rctl, b, w, lane, (unsigned)tag);
      if constexpr (j == A_COLS) job_cols<m>(J);
      sfor<3>([&](auto K) { event(IC<ev_at(A_S, j, m, K.value)>{}); });
      GAP();
    });
    if constexpr (j == A_TR || j == A_TR + 1) {
      if (mg == j - A_TR && job < 74) {
        const unsigned a = lds + A_JOB + (unsigned)(4 * (g >> 1) + (g & 1)) * 5120u + (t >> 2) * 256u + (t & 3) * 8u, sw = ((t >> 2) | ((g & 1) << 2)) << 5;
        u32x2 TR[10];
#pragma unroll
        for (int m = 0; m < 10; ++m) { const unsigned cb = 160u * w + 32u * (m >> 1); TR[m] = tr64(a + (m & 1) * 10240u + (cb >> 8) * 1024u + ((cb & 255u) ^ sw)); }
#pragma unroll
        for (int cb = 0; cb < 5; ++cb)
          st128_dev((u32x4){TR[2 * cb].x, TR[2 * cb].y, TR[2 * cb + 1].x, TR[2 * cb + 1].y}, Q.rq, Q.lv, (unsigned)(w * 74 + job) * 5120u + cb * 1024u);
      }
    }
    if constexpr (j == A_COLS + 1) { if (w == 1) job_pubc(lds + AX_JP, rctl, b, lane, (unsigned)tag); }
    if constexpr (j == A_CHK) { if (w == 3) check_wflags(p, lds + AX_FW, lane, tag); }
    if constexpr (j == J_GRAN) {
      if (w < 4) {
        const int tid = w * 64 + lane, c = tid < 240 ? tid : 0, sec = c / 80, cc = c - 80 * sec;
        *(LDSP float*)(size_t)(lds + AX_CS + tid * 4u) = colsum(lds + AX_G + sec * 1920u + cc * 8u, 640u, p.ws, sec * 640 + 80 * hg + cc, (unsigned)tag);
      }
    }
    if constexpr (j == J_GRAN + 1) {
      if (w == 7) *(LDSP u32x2*)(size_t)(lds + AX_Z) = (u32x2){0u, 0u};
      else if (w >= 4) {
        const int r0 = (w - 4) * 64 + lane, r = r0 < 160 ? r0 : 0;
        *(LDSP f32x2*)(size_t)(lds + AX_ST + r0 * 8u) = rowstat(lds + AX_G + 6144u + r * 16u, p.ws, par * 1024 + jt * 144 + r, (unsigned)tag);
      }
    }
    if constexpr (PRIO && lead) __builtin_amdgcn_s_setprio(0);
    GAP();
  });
  settle();
  stamp(2); cstamp(c1); sstamp(20);
  constexpr unsigned E_KB = 0, E_X = 2048, E_OT = E_X + 40960;
  auto pre = [&](auto K) { loadW<25 + K.value>(Wf, Q); };
  u32x4 QT[5], KT[6];
  {
    f32x4 c4[4]; f32x2 stt[5];
#pragma unroll
    for (int bb = 0; bb < 3; ++bb) c4[bb] = ld128f(lds + AX_CS + (bb * 80 + 20 * h + 4 * g) * 4u);
    c4[3] = ld128f(lds + AX_CS + ((h < 3 ? h : 0) * 80 + 20 * g + 16) * 4u);
#pragma unroll
    for (int a = 0; a < 5; ++a) stt[a] = ld64f(lds + AX_ST + (80 * mg + 16 * a + t) * 8u);
    const unsigned xw = lds + E_X + (unsigned)(5 * mg) * 4096u + h * 512u + t * 32u + g * 8u;
    sfor<5>([&](auto A) {
      constexpr int a = A.value;
      const f32x2 nm = bc2(-stt[a].x), rs = bc2(stt[a].y);
      f32x2 v[8];
#pragma unroll
      for (int bb = 0; bb < 4; ++bb) { v[2 * bb] = fma2(nm, lo2(c4[bb]), lo2(acc[bb][a])); v[2 * bb + 1] = fma2(nm, hi2(c4[bb]), hi2(acc[bb][a])); }
      pre(IC<2 * a>{}); GAP();
#pragma unroll
      for (int k = 0; k < 8; ++k) v[k] = v[k] * rs;
      pre(IC<2 * a + 1>{}); GAP();
      QT[a].x = pk2(v[0]); QT[a].y = pk2(v[1]); KT[a + 1].x = pk2(v[2]); KT[a + 1].y = pk2(v[3]);
      *(LDSP u32x2*)(size_t)(xw + a * 4096u) = (u32x2){pk2(v[4]), pk2(v[5])};
      *(LDSP u32x2*)(size_t)(xw + a * 4096u + 2048u) = (u32x2){pk2(v[6]), pk2(v[7])};
      { unsigned q0 = QT[a].x, q1 = QT[a].y, k0 = KT[a + 1].x, k1 = KT[a + 1].y; pin(q0); pin(q1); pin(k0); pin(k1); QT[a].x = q0; QT[a].y = q1; KT[a + 1].x = k0; KT[a + 1].y = k1; }
      GAP();
    });
    *(LDSP u32x2*)(size_t)(lds + E_KB + (mg ? 4096u + 43008u : 0u) + h * 512u + lane * 8u) = (u32x2){KT[5].x, KT[5].y};
  }
  sstamp(21);
  lds_bar();
  sstamp(22);
  const rsrc_t rh = mk(p.ws + WS_H, H_BYTES);
  {
    const unsigned xk = opaque(lds + E_X + (unsigned)(5 * mg) * 4096u + t * 32u + g * 8u + 5 * 512u);
    const unsigned xt = opaque(lds + E_X + (unsigned)(5 * mg) * 4096u + (unsigned)(4 * g + (t >> 2)) * 32u + (t & 3) * 8u + 6 * 512u);
    const unsigned xv2 = opaque(xt + h * 512u - 6 * 512u);
    const unsigned xv3 = opaque(xv2), xt3 = opaque(xt);
    const unsigned pb = mg ? 4096u : 0u;
    u32x4 VV[5], TT[5];
    { const u32x2 k0 = ld64(lds + E_KB + h * 512u + lane * 8u), k1 = ld64(xk - pb); KT[0] = (u32x4){k0.x, k0.y, k1.x, k1.y}; }
#pragma unroll
    for (int a = 0; a < 5; ++a) {
      const u32x2 q = ld64v(qz + a * qs), k = ld64v(xk + a * 4096u);
      QT[a].z = q.x; QT[a].w = q.y; KT[a + 1].z = k.x; KT[a + 1].w = k.y;
    }
#pragma unroll
    for (int a = 0; a < 5; ++a) {
      const u32x2 v0 = tr64(a ? xv2 + (a - 1) * 4096u : xv2 - pb), v1 = tr64(xv3 + a * 4096u), t0 = tr64(a ? xt + (a - 1) * 4096u : xt - pb), t1 = tr64(xt3 + a * 4096u);
      VV[a] = (u32x4){v0.x, v0.y, v1.x, v1.y}; TT[a] = (u32x4){t0.x, t0.y, t1.x, t1.y};
    }  GAP();
    const float c = 0.22360679775f * 1.44269504089f;
    const f32x4 z = {0.f, 0.f, 0.f, 0.f};
    f32x4 sp[5], sc[5];
#pragma unroll
    for (int a = 0; a < 5; ++a) { sp[a] = mfma(asb(KT[a]), asb(QT[a]), z); sc[a] = mfma(asb(KT[a + 1]), asb(QT[a]), z); }  pre(IC<10>{}); GAP();
    float s[5][4], mx[5], my[5];
    pin(dq);
#pragma unroll
    for (int r = 0; r < 4; ++r) vp[r] = dq >= -r;
#pragma unroll
    for (int a = 0; a < 5; ++a)
#pragma unroll
      for (int r = 0; r < 4; ++r) s[a][r] = vp[r] ? sp[a][r] : sc[a][r];  pre(IC<11>{}); GAP();
#pragma unroll
    for (int a = 0; a < 5; ++a)
#pragma unroll
      for (int r = 0; r < 4; ++r) s[a][r] = fmaf(s[a][r], c, a == 0 ? bias0[r] : bias[r]);  pre(IC<12>{}); GAP();
#pragma unroll
    for (int a = 0; a < 5; ++a) mx[a] = max3(s[a][0], s[a][1], s[a][2]);
    GAP();
#pragma unroll
    for (int a = 0; a < 5; ++a) mx[a] = max2(mx[a], s[a][3]);
    pre(IC<13>{}); GAP();
#pragma unroll
    for (int a = 0; a < 5; ++a) { auto r = __builtin_amdgcn_permlane16_swap(asu(mx[a]), asu(mx[a]), false, false); unsigned r0 = r[0], r1 = r[1]; mx[a] = asf(r0); my[a] = asf(r1); }  GAP();
#pragma unroll
    for (int a = 0; a < 5; ++a) mx[a] = max2(mx[a], my[a]);
    pre(IC<14>{}); GAP();
#pragma unroll
    for (int a = 0; a < 5; ++a) { auto r = __builtin_amdgcn_permlane32_swap(asu(mx[a]), asu(mx[a]), false, false); unsigned r0 = r[0], r1 = r[1]; mx[a] = asf(r0); my[a] = asf(r1); }
    GAP();
#pragma unroll
    for (int a = 0; a < 5; ++a) mx[a] = max2(mx[a], my[a]);  pre(IC<15>{}); GAP();
#pragma unroll
    for (int a = 0; a < 5; ++a)
#pragma unroll
      for (int r = 0; r < 4; ++r) s[a][r] -= mx[a];  pre(IC<16>{}); GAP();
#pragma unroll
    for (int a = 0; a < 5; ++a)
#pragma unroll
      for (int r = 0; r < 4; ++r) s[a][r] = __builtin_amdgcn_exp2f(s[a][r]);   pre(IC<17>{}); GAP();
    unsigned p0[5], p1[5];
#pragma unroll
    for (int a = 0; a < 5; ++a) { p0[a] = pk(s[a][0], s[a][1]); p1[a] = pk(s[a][2], s[a][3]); }  pre(IC<18>{}); GAP();
    f32x4 om[5], ot[5], ol[5];
    const bf16x8 ones = asb((u32x4){0x3f803f80u, 0x3f803f80u, 0x3f803f80u, 0x3f803f80u});
#pragma unroll
    for (int a = 0; a < 5; ++a) {
      const bf16x8 pf = asb((u32x4){p0[a] & mp0, p1[a] & mp1, p0[a] & ~mp0, p1[a] & ~mp1});
      ol[a] = mfma(ones, pf, z); om[a] = mfma(asb(VV[a]), pf, z); ot[a] = mfma(asb(TT[a]), pf, z);
    }  pre(IC<19>{}); GAP();
    float il[5];
#pragma unroll
    for (int a = 0; a < 5; ++a) il[a] = __builtin_amdgcn_rcpf(ol[a].x);
    GAP();
    f32x2 o2[5][4];
#pragma unroll
    for (int a = 0; a < 5; ++a) { const f32x2 i2 = bc2(il[a]); o2[a][0] = lo2(om[a]) * i2; o2[a][1] = hi2(om[a]) * i2; o2[a][2] = lo2(ot[a]) * i2; o2[a][3] = hi2(ot[a]) * i2; }  GAP();
    u32x2 O[6], Tl[5];
#pragma unroll
    for (int a = 0; a < 5; ++a) { O[a] = (u32x2){pk2(o2[a][0]), pk2(o2[a][1])}; Tl[a] = (u32x2){pk2(o2[a][2]), pk2(o2[a][3])}; }
    GAP();
    if (g == h) {
#pragma unroll
      for (int a = 0; a < 5; ++a) *(LDSP u32x2*)(size_t)(lds + E_OT + (5 * mg + a) * 512u + ((h & 1) * 16 + t) * 16u + (h >> 1) * 8u) = Tl[a];
    }
    O[5] = O[4];
    const int db = 5 * hg + h;
    const unsigned so = (db >> 1) * 4096u + (db & 1) * 512u;
#pragma unroll
    for (int ap = 0; ap < 3; ++ap) st128_dev(halffrag(O[2 * ap], O[2 * ap + 1]), rh, ov[ap], so);
  }
  sstamp(23);
  lds_bar();
  sstamp(24);
  {
    const int db = 5 * hg + 4;
    st128_dev(ld128u(lds + E_OT + (w < 5 ? w : 0) * 1024u + lane * 16u), rh, ov[3], (db >> 1) * 4096u + (db & 1) * 512u);
  }
  sstamp(25);
  wait_bar<0>();
  sstamp(26); sstamp(27); sstamp(28);
  if (w == 0 && lane == 0) put_flag(p.ws + WS_FH + myflag * 64u, tag);
  sfor<6>([&](auto R) { dmai_dev<(R.value % 5) * 1024u, R.value * 8192u>(Q.rq, Q.lv, Q.wo[R.value / 5], lw); });
  dmai_dev<3072u, 18 * 8192u>(Q.rq, Q.lv, Q.wo[3], lw); dmai_dev<4096u, 19 * 8192u>(Q.rq, Q.lv, Q.wo[3], lw);
  out_tile<9, 9>(p, ST, Q, Wf, mg, lds, w, lane, mt, n2, tag);
}

__global__ __launch_bounds__(512, 2) void fused_block(Params p0) {
  Stamps ST;
  stamp(9);
  sstamp(29);
  const int lane = threadIdx.x & 63;
  const int b = blockIdx.x, xcd = b & 7, i = b >> 3, par = xcd & 1, cgp = xcd >> 1;
  {
    u64 pc; asm volatile("s_getpc_b64 %0" : "=s"(pc));
    const unsigned o = min((unsigned)(i * 16 + (lane & 15)) * 128u, (unsigned)CODE_KB * 1024u);
    asm volatile("s_mov_b32 m0, %2\n\ts_nop 0\n\tglobal_load_lds_dword %0, %1" :: "v"(o), "s"(pc), "i"(LDS_BYTES - 256) : "memory");
  }
  extern __shared__ __attribute__((aligned(16))) char smem[];
  const unsigned lds = (unsigned)(size_t)(LDSP char*)smem;
  const int w = rfl(threadIdx.x >> 6);
  u64 tag;
  const int mt = MAP2 == 0 ? xcd * 4 + (i & 3) : MAP2 == 1 ? (xcd >> 1) * 8 + (i & 7) : (xcd >> 2) * 16 + (i & 15), n2 = MAP2 == 0 ? i >> 2 : MAP2 == 1 ? (xcd & 1) * 4 + (i >> 3) : (xcd & 3) * 2 + (i >> 4);
  if (i < 18) { const int u = 18 * cgp + i; ffn_tile(ST, lds, b, w, lane, par, u & 7, u >> 3, mt, n2, (par * 8 + (u & 7)) * 9 + (u >> 3), tag); }
  else { const int a = i - 18, hi = a >= 7, jt = a - 7 * hi, hg = 2 * cgp + hi; att_tile(ST, lds, b, w, lane, par, jt, hg, a * 8 + xcd, mt, n2, 144 + (par * 7 + jt) * 8 + hg, tag); }
}

extern "C" void kernel_launch(void* const* d_in, const int* in_sizes, int n_in, void* d_out, int out_size, void* d_ws, size_t ws_size, hipStream_t stream) {
  Params p;
  p.x = (const char*)d_in[0]; p.wqkv = (const char*)d_in[1]; p.wo = (const char*)d_in[2];
  p.w1 = (const char*)d_in[4]; p.w3 = (const char*)d_in[5]; p.w2 = (const char*)d_in[6];
  p.out = (char*)d_out; p.ws = (char*)d_ws;
  (void)hipFuncSetAttribute((const void*)fused_block, hipFuncAttributeMaxDynamicSharedMemorySize, LDS_BYTES);
  fused_block<<<256, 512, LDS_BYTES, stream>>>(p);
}
```
